```python
import torch

def _softmax(x, axis=-1):
    return torch.softmax(x, dim=axis)
def _gelu(x):
    return torch.nn.functional.gelu(x, approximate="tanh")
def _silu(x):
    return torch.nn.functional.silu(x)
def _sigmoid(x):
    return torch.sigmoid(x)
def _layernorm(x, axis=-1, eps=1e-5):
    m = x.mean(dim=axis, keepdim=True)
    v = x.var(dim=axis, keepdim=True, unbiased=False)
    return (x - m) / torch.sqrt(v + eps)
def _scatter_add(tgt, idx, src, axis=0):
    return tgt.clone().index_add_(axis, idx, src)
def _segment_sum(src, seg, n):
    out = torch.zeros((n,) + src.shape[1:], dtype=src.dtype)
    return out.index_add_(0, seg, src)
def _pad(x, pw):
    flat = tuple(v for pair in reversed(pw) for v in pair)
    return torch.nn.functional.pad(x, flat)

kernel_name = 'gated_mix__mha_block_cached__moe_arch_mod__decode'

def setup_inputs():

    _w = (lambda a: (lambda t: t.to(torch.bfloat16) if t.is_floating_point() else t)(torch.as_tensor(a)))
    def _A_setup():
        B, Sq, D = 8, 1, 512
        H, Sk, Dh = 32, 4096, 128
        return {
            "x": _w((lambda *s: torch.randn(*s, dtype=torch.bfloat16) * 0.05)(B, Sq, D)),
            "k_cache": _w((lambda *s: torch.randn(*s, dtype=torch.bfloat16) * 3.2)(B, H, Sk, Dh)),
            "v_cache": _w((lambda *s: torch.randn(*s, dtype=torch.bfloat16) * 1.6)(B, H, Sk, Dh)),
            "wq": _w((lambda *s: torch.randn(*s, dtype=torch.bfloat16) * 3.2)(D, H * Dh) * (D ** -0.5)),
            "wo": _w((lambda *s: torch.randn(*s, dtype=torch.bfloat16) * 1.6)(H * Dh, D) * ((H * Dh) ** -0.5)),
        }
    _A_in = _A_setup()

    def _B_setup():
        B, S, D, F, C = 8, 1, 512, 1792, 1
        return {
            "x": _w((lambda *s: torch.randn(*s, dtype=torch.bfloat16) * 0.05)(B, S, D)),
            "w_r": _w((lambda *s: torch.randn(*s, dtype=torch.bfloat16) * 1.6)(D)),
            "w_gate": _w((lambda *s: torch.randn(*s, dtype=torch.bfloat16) * 0.1)(D, F)),
            "w_up": _w((lambda *s: torch.randn(*s, dtype=torch.bfloat16) * 0.1)(D, F)),
            "w_down": _w((lambda *s: torch.randn(*s, dtype=torch.bfloat16) * 0.1)(F, D)),
        }
    _B_in = _B_setup()

    return {
        "A_x": _A_in["x"],
        "A_k_cache": _A_in["k_cache"],
        "A_v_cache": _A_in["v_cache"],
        "A_wq": _A_in["wq"],
        "A_wo": _A_in["wo"],
        "B_x": _B_in["x"],
        "B_w_r": _B_in["w_r"],
        "B_w_gate": _B_in["w_gate"],
        "B_w_up": _B_in["w_up"],
        "B_w_down": _B_in["w_down"],
        "gate_w": _w((lambda *s: torch.randn(*s, dtype=torch.bfloat16) * 0.05)(512) * 16.0),
    }

def reference(A_x, A_k_cache, A_v_cache, A_wq, A_wo, B_x, B_w_r, B_w_gate, B_w_up, B_w_down, gate_w):
    def _A_ref(x, k_cache, v_cache, wq, wo):
        B, Sq, D = x.shape
        H, Dh = 32, 128
        q = (lambda x, *d: x.permute(*d))((x @ wq).reshape(B, Sq, H, Dh), 0, 2, 1, 3)
        scores = torch.einsum("bhqd,bhkd->bhqk", q, k_cache) * (Dh ** -0.5)
        p = _softmax(scores, axis=-1)
        o = torch.einsum("bhqk,bhkd->bhqd", p, v_cache)
        return (lambda x, *d: x.permute(*d))(o, 0, 2, 1, 3).reshape(B, Sq, H * Dh) @ wo

    def _B_ref(x, w_r, w_gate, w_up, w_down):
        B, S, D = x.shape
        C = 1
        r = x @ w_r
        idx = torch.argsort(-r, axis=-1)[:, :C]
        idx_e = (lambda x, n, axis: torch.repeat_interleave(x, n, dim=axis))(idx.reshape(B, C, 1), D, axis=2)
        x_sel = (lambda x, i, axis: torch.take_along_dim(x, i, dim=axis))(x, idx_e, axis=1)
        g = _silu(x_sel @ w_gate)
        u = x_sel @ w_up
        y = (g * u) @ w_down
        r_sel = _sigmoid((lambda x, i, axis: torch.take_along_dim(x, i, dim=axis))(r, idx, axis=1))
        y = y * r_sel.reshape(B, C, 1)
        b_off = (torch.arange(B) * S).reshape(B, 1)
        flat_idx = (idx + b_off).reshape(B * C)
        zero = (x * 0.0).reshape(B * S, D)
        upd = _scatter_add(zero, flat_idx, y.reshape(B * C, D), axis=0)
        return x + upd.reshape(B, S, D)

    x = A_x
    g = _sigmoid((x * gate_w).sum(-1, keepdims=True))
    a = _A_ref(x, A_k_cache, A_v_cache, A_wq, A_wo)
    b = _B_ref(x, B_w_r, B_w_gate, B_w_up, B_w_down)
    return g * a + (1 - g) * b
```

```cpp
#include <hip/hip_runtime.h>
#include <stdint.h>
#include <type_traits>

#define GAS __attribute__((address_space(1)))
#define LAS __attribute__((address_space(3)))
typedef unsigned short u16;
typedef unsigned v4u __attribute__((ext_vector_type(4)));
typedef unsigned v2u __attribute__((ext_vector_type(2)));
typedef float f32x4 __attribute__((ext_vector_type(4)));
typedef float f32x2 __attribute__((ext_vector_type(2)));
typedef __bf16 bf16x2 __attribute__((ext_vector_type(2)));

namespace {
constexpr int NB = 8, NH = 32, SK = 4096, DH = 128, DM = 512, FF = 1792, FS = FF / NH;
constexpr int NMAIN = 8, NTHR = 64 * (NMAIN + 1);
constexpr float SM_C = 0.088388347648318440f * 1.4426950408889634f;

constexpr int L_QPART = 0;
constexpr int L_SC = L_QPART + 8 * 128 * 4;
constexpr int L_PART = L_SC + 8 * 512 * 4;
constexpr int L_ML = L_PART + 8 * 128 * 4;
constexpr int L_YP = L_ML + 128;
constexpr int L_YMOE = L_YP + 8 * 512 * 4;
constexpr int L_XS = L_YMOE + 512 * 4;
constexpr int L_ACT = L_XS + 512 * 4;
constexpr int L_SCAL = L_ACT + 256;
constexpr int L_RED = L_SCAL + 64;
constexpr int L_END = L_RED + 4 * 512 * 4;
constexpr int LDS_BYTES = 96 * 1024;
static_assert(L_END <= LDS_BYTES, "lds");

constexpr size_t WS_CNT = 0;
constexpr size_t WS_CNT_BYTES = 1024;
constexpr size_t WS_PART = 4096;

__device__ __forceinline__ float u2f(unsigned u) { return __builtin_bit_cast(float, u); }
__device__ __forceinline__ float bflo(unsigned u) { return __builtin_bit_cast(float, u << 16); }
__device__ __forceinline__ float bfhi(unsigned u) { return __builtin_bit_cast(float, u & 0xffff0000u); }
__device__ __forceinline__ float dot2(unsigned a, unsigned b, float acc) {
  return __builtin_amdgcn_fdot2_f32_bf16(__builtin_bit_cast(bf16x2, a), __builtin_bit_cast(bf16x2, b), acc, false);
}
__device__ __forceinline__ unsigned pack_bf16(float lo, float hi) {
  bf16x2 v = {(__bf16)lo, (__bf16)hi};
  return __builtin_bit_cast(unsigned, v);
}
__device__ __forceinline__ float rbf(float x) { return __builtin_bit_cast(float, (unsigned)__builtin_bit_cast(u16, (__bf16)x) << 16); }
template <int CTRL> __device__ __forceinline__ float dpp(float x) {
  return __builtin_bit_cast(float, __builtin_amdgcn_mov_dpp(__builtin_bit_cast(int, x), CTRL, 0xf, 0xf, true));
}
constexpr int XOR1 = 0xB1, XOR2 = 0x4E, XOR7 = 0x141, XOR8 = 0x128;
__device__ __forceinline__ float xrow_sum(float x) {
  auto s = __builtin_amdgcn_permlane16_swap(__builtin_bit_cast(unsigned, x), __builtin_bit_cast(unsigned, x), false, false);
  x = u2f(s[0]) + u2f(s[1]);
  auto t = __builtin_amdgcn_permlane32_swap(__builtin_bit_cast(unsigned, x), __builtin_bit_cast(unsigned, x), false, false);
  return u2f(t[0]) + u2f(t[1]);
}
__device__ __forceinline__ float xrow_max(float x) {
  auto s = __builtin_amdgcn_permlane16_swap(__builtin_bit_cast(unsigned, x), __builtin_bit_cast(unsigned, x), false, false);
  x = fmaxf(u2f(s[0]), u2f(s[1]));
  auto t = __builtin_amdgcn_permlane32_swap(__builtin_bit_cast(unsigned, x), __builtin_bit_cast(unsigned, x), false, false);
  return fmaxf(u2f(t[0]), u2f(t[1]));
}
__device__ __forceinline__ float row_sum(float x) { x += dpp<XOR8>(x); x += dpp<XOR7>(x); x += dpp<XOR2>(x); x += dpp<XOR1>(x); return x; }
__device__ __forceinline__ float row_max(float x) { x = fmaxf(x, dpp<XOR8>(x)); x = fmaxf(x, dpp<XOR7>(x)); x = fmaxf(x, dpp<XOR2>(x)); x = fmaxf(x, dpp<XOR1>(x)); return x; }
__device__ __forceinline__ float wave_sum(float x) { return xrow_sum(row_sum(x)); }
__device__ __forceinline__ float wave_max(float x) { return xrow_max(row_max(x)); }
__device__ __forceinline__ float dot8(const v4u k, const v4u q) {
  float d = dot2(k.x, q.x, 0.f); d = dot2(k.y, q.y, d); d = dot2(k.z, q.z, d); d = dot2(k.w, q.w, d); return d;
}
__device__ __forceinline__ float treduce16(float (&x)[16], int c) {
#define TR_STEP(HALF, CTRL, BIT) _Pragma("unroll") for (int h = 0; h < (HALF); ++h) { \
    const float lo_ = x[h], hi_ = x[h + (HALF)]; const float t1_ = lo_ + dpp<CTRL>(lo_), t2_ = hi_ + dpp<CTRL>(hi_); x[h] = (c & (BIT)) ? t2_ : t1_; }
  TR_STEP(8, XOR8, 8) TR_STEP(4, XOR7, 4) TR_STEP(2, XOR2, 2) TR_STEP(1, XOR1, 1)
#undef TR_STEP
  return x[0];
}
template <int T> __device__ __forceinline__ float bcast16(float p) { return dpp<0x150 + T>(p); }
__device__ __forceinline__ v4u ldnt(const GAS v4u* p) { return __builtin_nontemporal_load(p); }
__device__ __forceinline__ float sigmoidf_(float v) { return 1.0f / (1.0f + __expf(-v)); }

template <int T> __device__ __forceinline__ void pv_step(f32x2 (&acc)[4], const v4u v, float p) {
  const float pb = bcast16<T>(p);
  const f32x2 p2 = {pb, pb};
#pragma unroll
  for (int j = 0; j < 4; ++j) acc[j] = __builtin_elementwise_fma(p2, f32x2{bflo(v[j]), bfhi(v[j])}, acc[j]);
  asm volatile("" : "+v"(acc[0]), "+v"(acc[1]), "+v"(acc[2]), "+v"(acc[3]));
}
__device__ __forceinline__ v4u ldb_nt(__amdgpu_buffer_rsrc_t rs, int voff, int soff) { return __builtin_amdgcn_raw_buffer_load_b128(rs, voff, soff, 2); }
template <int T, int N> struct Unroll { template <class F> static __device__ __forceinline__ void run(F&& f) { f(std::integral_constant<int, T>{}); Unroll<T + 1, N>::run(f); } };
template <int N> struct Unroll<N, N> { template <class F> static __device__ __forceinline__ void run(F&&) {} };

__global__ void __launch_bounds__(NTHR) fused_kernel(const u16* __restrict__ xg, const u16* __restrict__ Kg, const u16* __restrict__ Vg, const u16* __restrict__ wq,
                                                     const u16* __restrict__ wo, const u16* __restrict__ w_r, const u16* __restrict__ w_gate, const u16* __restrict__ w_up,
                                                     const u16* __restrict__ w_down, const u16* __restrict__ gate_w, u16* __restrict__ out, unsigned char* __restrict__ ws) {
  extern __shared__ __attribute__((aligned(16))) unsigned char smem[];
  LAS unsigned char* L = (LAS unsigned char*)smem;
  const int tid = threadIdx.x, lane = tid & 63, w = __builtin_amdgcn_readfirstlane(tid >> 6);
  const int bid = blockIdx.x;
  const int h = (bid & 7) * 4 + (bid >> 6), b = (bid >> 3) & 7;
  const int r = lane >> 4, c = lane & 15;

  if (w < NMAIN) {
    const GAS v4u* xp = (const GAS v4u*)(xg + b * DM + 64 * w + 16 * r);
    const v4u xa = xp[0], xb = xp[1];
    const GAS v4u* wqp = (const GAS v4u*)(wq + (size_t)(64 * w + 16 * r) * (NH * DH) + h * DH + 8 * c);
    v4u W[16];
#pragma unroll
    for (int t = 0; t < 16; ++t) W[t] = wqp[(size_t)t * (NH * DH / 8)];
    const size_t kvoff = (size_t)(b * NH + h) * SK * DH;
    const __amdgpu_buffer_rsrc_t rsK = __builtin_amdgcn_make_buffer_rsrc((void*)(Kg + kvoff), 0, SK * DH * 2, 0x00020000);
    const __amdgpu_buffer_rsrc_t rsV = __builtin_amdgcn_make_buffer_rsrc((void*)(Vg + kvoff), 0, SK * DH * 2, 0x00020000);
    const int voff = w * 1024 + lane * 16;
    v4u P[16];
#pragma unroll
    for (int t = 0; t < 16; ++t) P[t] = ldb_nt(rsK, voff, t * 8192);
    __builtin_amdgcn_sched_barrier(0);
    float qa[8];
#pragma unroll
    for (int j = 0; j < 8; ++j) qa[j] = 0.f;
#pragma unroll
    for (int t = 0; t < 16; ++t) {
      const unsigned xw = (t < 8) ? xa[t >> 1] : xb[(t - 8) >> 1];
      const float xv = (t & 1) ? bfhi(xw) : bflo(xw);
#pragma unroll
      for (int j = 0; j < 4; ++j) { qa[2 * j] = fmaf(xv, bflo(W[t][j]), qa[2 * j]); qa[2 * j + 1] = fmaf(xv, bfhi(W[t][j]), qa[2 * j + 1]); }
    }
#pragma unroll
    for (int j = 0; j < 8; ++j) qa[j] = xrow_sum(qa[j]);
    if (r == 0) {
      LAS f32x4* qp = (LAS f32x4*)(L + L_QPART) + w * 32 + c * 2;
      qp[0] = f32x4{qa[0], qa[1], qa[2], qa[3]}; qp[1] = f32x4{qa[4], qa[5], qa[6], qa[7]};
    }
    __syncthreads();
    v4u q;
    {
      f32x4 s0 = {0.f, 0.f, 0.f, 0.f}, s1 = {0.f, 0.f, 0.f, 0.f};
#pragma unroll
      for (int ww = 0; ww < 8; ++ww) { const LAS f32x4* qp = (const LAS f32x4*)(L + L_QPART) + ww * 32 + c * 2; s0 += qp[0]; s1 += qp[1]; }
      q = v4u{pack_bf16(s0[0], s0[1]), pack_bf16(s0[2], s0[3]), pack_bf16(s1[0], s1[1]), pack_bf16(s1[2], s1[3])};
    }
    const GAS v4u* wop = (const GAS v4u*)(wo + (size_t)(h * DH + 16 * w) * DM + lane * 8);
    v4u Wo[16];
#pragma unroll
    for (int t = 0; t < 16; ++t) Wo[t] = wop[t * (DM / 8)];
    __builtin_amdgcn_sched_barrier(0);
    LAS float* sc = (LAS float*)(L + L_SC) + w * 512 + lane;
    v4u R[4];
    float x[16];
    float mx;
#pragma unroll
    for (int t = 0; t < 16; ++t) {
      x[t] = dot8(P[t], q);
      if (t >= 12) R[t - 12] = ldb_nt(rsK, voff, (16 + t - 12) * 8192);
      __builtin_amdgcn_sched_barrier(0);
    }
    { const float s = treduce16(x, c); sc[0] = s; mx = s; }
    for (int g = 1; g < 7; ++g) {
      const int so = (16 * g + 4) * 8192;
#pragma unroll
      for (int t = 0; t < 16; ++t) {
        x[t] = dot8(R[t & 3], q);
        R[t & 3] = ldb_nt(rsK, voff, so + t * 8192);
        __builtin_amdgcn_sched_barrier(0);
      }
      const float s = treduce16(x, c); sc[g * 64] = s; mx = fmaxf(mx, s);
    }
    {
      const int so = (16 * 7 + 4) * 8192;
#pragma unroll
      for (int t = 0; t < 16; ++t) {
        x[t] = dot8(R[t & 3], q);
        R[t & 3] = (t < 12) ? ldb_nt(rsK, voff, so + t * 8192) : ldb_nt(rsV, voff, (t - 12) * 8192);
        __builtin_amdgcn_sched_barrier(0);
      }
      const float s = treduce16(x, c); sc[7 * 64] = s; mx = fmaxf(mx, s);
    }
    const float m = wave_max(mx);
    unsigned Wp[8][8];
#pragma unroll
    for (int i = 0; i < 8; ++i)
#pragma unroll
      for (int j = 0; j < 4; ++j) {
        Wp[i][2 * j] = __builtin_amdgcn_perm(Wo[2 * i + 1][j], Wo[2 * i][j], 0x05040100u);
        Wp[i][2 * j + 1] = __builtin_amdgcn_perm(Wo[2 * i + 1][j], Wo[2 * i][j], 0x07060302u);
        asm volatile("" : "+v"(Wp[i][2 * j]), "+v"(Wp[i][2 * j + 1]));
      }
    f32x2 acc2[4];
#pragma unroll
    for (int j = 0; j < 4; ++j) acc2[j] = f32x2{0.f, 0.f};
    float lsum = 0.f;
    const float mc = m * SM_C;
    for (int g = 0; g < 7; ++g) {
      const float p = __builtin_amdgcn_exp2f(fmaf(sc[g * 64], SM_C, -mc));
      lsum += p;
      const int so = (16 * g + 4) * 8192;
      Unroll<0, 16>::run([&](auto T) {
        constexpr int t = decltype(T)::value;
        pv_step<t>(acc2, R[t & 3], p);
        R[t & 3] = ldb_nt(rsV, voff, so + t * 8192);
        __builtin_amdgcn_sched_barrier(0);
      });
    }
    {
      const float p = __builtin_amdgcn_exp2f(fmaf(sc[7 * 64], SM_C, -mc));
      lsum += p;
      const int so = (16 * 7 + 4) * 8192;
      Unroll<0, 16>::run([&](auto T) {
        constexpr int t = decltype(T)::value;
        pv_step<t>(acc2, R[t & 3], p);
        if (t < 12) R[t & 3] = ldb_nt(rsV, voff, so + t * 8192);
        __builtin_amdgcn_sched_barrier(0);
      });
    }
    float acc[8];
#pragma unroll
    for (int j = 0; j < 8; ++j) acc[j] = xrow_sum(acc2[j >> 1][j & 1]);
    lsum = wave_sum(lsum);
    if (r == 0) {
      LAS f32x4* pp = (LAS f32x4*)(L + L_PART) + w * 32 + c * 2;
      pp[0] = f32x4{acc[0], acc[1], acc[2], acc[3]}; pp[1] = f32x4{acc[4], acc[5], acc[6], acc[7]};
    }
    if (lane == 0) { LAS float* ml = (LAS float*)(L + L_ML); ml[2 * w] = m; ml[2 * w + 1] = lsum; }
    __syncthreads();
    {
      const LAS float* ml = (const LAS float*)(L + L_ML);
      float M = ml[0];
#pragma unroll
      for (int ww = 1; ww < 8; ++ww) M = fmaxf(M, ml[2 * ww]);
      float Lsum = 0.f;
#pragma unroll
      for (int ww = 0; ww < 8; ++ww) Lsum += __builtin_amdgcn_exp2f((ml[2 * ww] - M) * SM_C) * ml[2 * ww + 1];
      const LAS float* part = (const LAS float*)(L + L_PART);
      const float e0 = __builtin_amdgcn_exp2f((ml[4 * r] - M) * SM_C), e1 = __builtin_amdgcn_exp2f((ml[4 * r + 2] - M) * SM_C);
      float o = e0 * part[(2 * r) * 128 + 16 * w + c] + e1 * part[(2 * r + 1) * 128 + 16 * w + c];
      o = xrow_sum(o) / Lsum;
      const unsigned op = pack_bf16(o, dpp<XOR1>(o));
      float y[8];
#pragma unroll
      for (int j = 0; j < 8; ++j) y[j] = 0.f;
#pragma unroll
      for (int i = 0; i < 8; ++i) {
        const unsigned os = __builtin_amdgcn_readlane(op, 2 * i);
#pragma unroll
        for (int j = 0; j < 8; ++j) y[j] = dot2(Wp[i][j], os, y[j]);
      }
      const float g = ((const LAS float*)(L + L_SCAL))[0];
      LAS f32x4* yp = (LAS f32x4*)(L + L_YP) + w * 128 + lane * 2;
      yp[0] = f32x4{y[0] * g, y[1] * g, y[2] * g, y[3] * g}; yp[1] = f32x4{y[4] * g, y[5] * g, y[6] * g, y[7] * g};
    }
  } else {
    const GAS v4u* xp = (const GAS v4u*)(xg + b * DM + 8 * lane);
    const v4u xv = xp[0];
    const v4u gw = *(const GAS v4u*)(gate_w + 8 * lane);
    const v4u wr = *(const GAS v4u*)(w_r + 8 * lane);
    const int rr = lane >> 3, cc = lane & 7, ccl = cc < 7 ? cc : 6;
    const GAS v4u* gp = (const GAS v4u*)(w_gate + (size_t)(64 * rr) * FF + h * FS + 8 * ccl);
    const GAS v4u* up = (const GAS v4u*)(w_up + (size_t)(64 * rr) * FF + h * FS + 8 * ccl);
    v4u G[8], U[8];
#pragma unroll
    for (int t = 0; t < 8; ++t) { G[t] = gp[t * (FF / 8)]; U[t] = up[t * (FF / 8)]; }
    __builtin_amdgcn_sched_barrier(0);
    __syncthreads();
    LAS float* xs = (LAS float*)(L + L_XS);
    { LAS f32x4* xs4 = (LAS f32x4*)xs + 2 * lane;
      xs4[0] = f32x4{bflo(xv.x), bfhi(xv.x), bflo(xv.y), bfhi(xv.y)}; xs4[1] = f32x4{bflo(xv.z), bfhi(xv.z), bflo(xv.w), bfhi(xv.w)}; }
    float sg = 0.f, sr = 0.f;
#pragma unroll
    for (int j = 0; j < 4; ++j) {
      sg += rbf(bflo(xv[j]) * bflo(gw[j])); sg += rbf(bfhi(xv[j]) * bfhi(gw[j]));
      sr = fmaf(bflo(xv[j]), bflo(wr[j]), sr); sr = fmaf(bfhi(xv[j]), bfhi(wr[j]), sr);
    }
    sg = rbf(wave_sum(sg)); sr = rbf(wave_sum(sr));
    const float g = rbf(sigmoidf_(sg)), rs = rbf(sigmoidf_(sr)), omg = rbf(1.0f - g);
    if (lane == 0) { LAS float* sca = (LAS float*)(L + L_SCAL); sca[0] = g; sca[1] = omg; }
    float ag[8], au[8];
#pragma unroll
    for (int j = 0; j < 8; ++j) { ag[j] = 0.f; au[j] = 0.f; }
    for (int it = 0; it < 8; ++it) {
      v4u Gc[8], Uc[8];
#pragma unroll
      for (int t = 0; t < 8; ++t) { Gc[t] = G[t]; Uc[t] = U[t]; }
      if (it < 7) {
#pragma unroll
        for (int t = 0; t < 8; ++t) { G[t] = gp[((it + 1) * 8 + t) * (FF / 8)]; U[t] = up[((it + 1) * 8 + t) * (FF / 8)]; }
      }
      const f32x4 xq0 = *(const LAS f32x4*)(xs + 64 * rr + 8 * it), xq1 = *(const LAS f32x4*)(xs + 64 * rr + 8 * it + 4);
#pragma unroll
      for (int t = 0; t < 8; ++t) {
        const float xt = t < 4 ? xq0[t & 3] : xq1[t & 3];
#pragma unroll
        for (int j = 0; j < 4; ++j) {
          ag[2 * j] = fmaf(xt, bflo(Gc[t][j]), ag[2 * j]); ag[2 * j + 1] = fmaf(xt, bfhi(Gc[t][j]), ag[2 * j + 1]);
          au[2 * j] = fmaf(xt, bflo(Uc[t][j]), au[2 * j]); au[2 * j + 1] = fmaf(xt, bfhi(Uc[t][j]), au[2 * j + 1]);
        }
      }
    }
    const GAS v4u* dp = (const GAS v4u*)(w_down + (size_t)(h * FS) * DM + lane * 8);
    v4u Dn[14];
#pragma unroll
    for (int t = 0; t < 14; ++t) Dn[t] = dp[t * (DM / 8)];
    LAS float* act = (LAS float*)(L + L_ACT);
#pragma unroll
    for (int j = 0; j < 8; ++j) {
      float gg = ag[j], uu = au[j];
      gg += dpp<XOR8>(gg); uu += dpp<XOR8>(uu);
      gg = rbf(xrow_sum(gg)); uu = rbf(xrow_sum(uu));
      const float si = rbf(gg * sigmoidf_(gg));
      if (lane < 7) act[8 * lane + j] = rbf(si * uu);
    }
    float y[8];
#pragma unroll
    for (int j = 0; j < 8; ++j) y[j] = 0.f;
    for (int it = 0; it < 4; ++it) {
      v4u Dc[14];
#pragma unroll
      for (int t = 0; t < 14; ++t) Dc[t] = Dn[t];
      if (it < 3) {
#pragma unroll
        for (int t = 0; t < 14; ++t) Dn[t] = dp[((it + 1) * 14 + t) * (DM / 8)];
      }
#pragma unroll
      for (int t = 0; t < 14; ++t) {
        const float a = act[it * 14 + t];
#pragma unroll
        for (int j = 0; j < 4; ++j) { y[2 * j] = fmaf(a, bflo(Dc[t][j]), y[2 * j]); y[2 * j + 1] = fmaf(a, bfhi(Dc[t][j]), y[2 * j + 1]); }
      }
    }
    const float f = omg * rs;
    LAS f32x4* ym = (LAS f32x4*)(L + L_YMOE) + 2 * lane;
    ym[0] = f32x4{y[0] * f, y[1] * f, y[2] * f, y[3] * f}; ym[1] = f32x4{y[4] * f, y[5] * f, y[6] * f, y[7] * f};
    __syncthreads();
  }
  __syncthreads();
  const __amdgpu_buffer_rsrc_t rs_part = __builtin_amdgcn_make_buffer_rsrc((void*)(ws + WS_PART), 0, NB * NH * DM * 4, 0x00020000);
  if (tid < 128) {
    f32x4 s = ((const LAS f32x4*)(L + L_YMOE))[tid];
#pragma unroll
    for (int ww = 0; ww < 8; ++ww) s += ((const LAS f32x4*)(L + L_YP))[ww * 128 + tid];
    __builtin_amdgcn_raw_buffer_store_b128(__builtin_bit_cast(v4u, s), rs_part, ((b * NH + h) * DM + tid * 4) * 4, 0, 16);
    asm volatile("s_waitcnt vmcnt(0)" ::: "memory");
  }
  __syncthreads();
  volatile LAS unsigned* flag = (volatile LAS unsigned*)(L + L_SCAL + 16);
  if (tid == 0) {
    const unsigned old = __hip_atomic_fetch_add((GAS unsigned*)(ws + WS_CNT) + b * 32, 1u, __ATOMIC_RELAXED, __HIP_MEMORY_SCOPE_AGENT);
    __builtin_amdgcn_fence(__ATOMIC_ACQUIRE, "wavefront");
    *flag = (old == NH - 1) ? 1u : 0u;
  }
  __syncthreads();
  if (*flag == 0u) return;
  if (tid < 512) {
    const int cg = tid & 127, pg = tid >> 7;
    v4u t[8];
#pragma unroll
    for (int i = 0; i < 8; ++i) t[i] = __builtin_amdgcn_raw_buffer_load_b128(rs_part, ((b * NH + pg * 8 + i) * DM + cg * 4) * 4, 0, 16);
    f32x4 s = {0.f, 0.f, 0.f, 0.f};
#pragma unroll
    for (int i = 0; i < 8; ++i) s += __builtin_bit_cast(f32x4, t[i]);
    ((LAS f32x4*)(L + L_RED))[pg * 128 + cg] = s;
  }
  __syncthreads();
  if (tid < 128) {
    f32x4 s = ((const LAS f32x4*)(L + L_RED))[tid];
#pragma unroll
    for (int i = 1; i < 4; ++i) s += ((const LAS f32x4*)(L + L_RED))[i * 128 + tid];
    const float omg = ((const LAS float*)(L + L_SCAL))[1];
    const v2u xv = *(const GAS v2u*)(xg + b * DM + tid * 4);
    v2u o;
    o.x = pack_bf16(fmaf(omg, bflo(xv.x), s[0]), fmaf(omg, bfhi(xv.x), s[1]));
    o.y = pack_bf16(fmaf(omg, bflo(xv.y), s[2]), fmaf(omg, bfhi(xv.y), s[3]));
    *(GAS v2u*)(out + b * DM + tid * 4) = o;
  }
}
}

extern "C" void kernel_launch(void* const* d_in, const int* in_sizes, int n_in, void* d_out, int out_size, void* d_ws, size_t ws_size, hipStream_t stream) {
  (void)in_sizes; (void)n_in; (void)out_size; (void)ws_size;
  hipFuncSetAttribute((const void*)fused_kernel, hipFuncAttributeMaxDynamicSharedMemorySize, LDS_BYTES);
  hipMemsetAsync((unsigned char*)d_ws + WS_CNT, 0, WS_CNT_BYTES, stream);
  fused_kernel<<<dim3(NB * NH), dim3(NTHR), LDS_BYTES, stream>>>(
      (const u16*)d_in[0], (const u16*)d_in[1], (const u16*)d_in[2], (const u16*)d_in[3], (const u16*)d_in[4],
      (const u16*)d_in[6], (const u16*)d_in[7], (const u16*)d_in[8], (const u16*)d_in[9], (const u16*)d_in[10], (u16*)d_out, (unsigned char*)d_ws);
}
```

```cpp
#include <hip/hip_runtime.h>
#include <stdint.h>
#include <type_traits>

#define GAS __attribute__((address_space(1)))
#define LAS __attribute__((address_space(3)))
typedef unsigned short u16;
typedef unsigned v4u __attribute__((ext_vector_type(4)));
typedef unsigned v2u __attribute__((ext_vector_type(2)));
typedef float f32x4 __attribute__((ext_vector_type(4)));
typedef float f32x2 __attribute__((ext_vector_type(2)));
typedef __bf16 bf16x2 __attribute__((ext_vector_type(2)));

#define STAMP(i)
#define MOE_IT1 8
#define MOE_IT2 4
#define LDW(p) (*(p))
namespace {
constexpr int NB = 8, NH = 32, SK = 4096, DH = 128, DM = 512, FF = 1792, FS = FF / NH;
constexpr int NMAIN = 8, NTHR = 64 * (NMAIN + 1);
constexpr float SM_C = 0.088388347648318440f * 1.4426950408889634f;

constexpr int L_QPART = 0;
constexpr int L_SC = L_QPART + 8 * 128 * 4;
constexpr int L_PART = L_SC + 8 * 512 * 4;
constexpr int L_ML = L_PART + 8 * 128 * 4;
constexpr int L_YP = L_ML + 128;
constexpr int L_YMOE = L_YP + 8 * 512 * 4;
constexpr int L_XS = L_YMOE + 512 * 4;
constexpr int L_ACT = L_XS + 512 * 4;
constexpr int L_SCAL = L_ACT + 256;
constexpr int L_RED = L_SCAL + 64;
constexpr int L_END = L_RED + 4 * 512 * 4;
constexpr int LDS_BYTES = 96 * 1024;
static_assert(L_END <= LDS_BYTES, "lds");

constexpr size_t WS_CNT = 0;
constexpr size_t WS_CNT_BYTES = 1024;
constexpr size_t WS_PART = 4096;

__device__ __forceinline__ float u2f(unsigned u) { return __builtin_bit_cast(float, u); }
__device__ __forceinline__ float bflo(unsigned u) { return __builtin_bit_cast(float, u << 16); }
__device__ __forceinline__ float bfhi(unsigned u) { return __builtin_bit_cast(float, u & 0xffff0000u); }
__device__ __forceinline__ float dot2(unsigned a, unsigned b, float acc) {
  return __builtin_amdgcn_fdot2_f32_bf16(__builtin_bit_cast(bf16x2, a), __builtin_bit_cast(bf16x2, b), acc, false);
}
__device__ __forceinline__ unsigned pack_bf16(float lo, float hi) {
  bf16x2 v = {(__bf16)lo, (__bf16)hi};
  return __builtin_bit_cast(unsigned, v);
}
__device__ __forceinline__ float rbf(float x) { return __builtin_bit_cast(float, (unsigned)__builtin_bit_cast(u16, (__bf16)x) << 16); }
template <int CTRL> __device__ __forceinline__ float dpp(float x) {
  return __builtin_bit_cast(float, __builtin_amdgcn_mov_dpp(__builtin_bit_cast(int, x), CTRL, 0xf, 0xf, true));
}
constexpr int XOR1 = 0xB1, XOR2 = 0x4E, XOR7 = 0x141, XOR8 = 0x128;
__device__ __forceinline__ float xrow_sum(float x) {
  auto s = __builtin_amdgcn_permlane16_swap(__builtin_bit_cast(unsigned, x), __builtin_bit_cast(unsigned, x), false, false);
  x = u2f(s[0]) + u2f(s[1]);
  auto t = __builtin_amdgcn_permlane32_swap(__builtin_bit_cast(unsigned, x), __builtin_bit_cast(unsigned, x), false, false);
  return u2f(t[0]) + u2f(t[1]);
}
__device__ __forceinline__ float xrow_max(float x) {
  auto s = __builtin_amdgcn_permlane16_swap(__builtin_bit_cast(unsigned, x), __builtin_bit_cast(unsigned, x), false, false);
  x = fmaxf(u2f(s[0]), u2f(s[1]));
  auto t = __builtin_amdgcn_permlane32_swap(__builtin_bit_cast(unsigned, x), __builtin_bit_cast(unsigned, x), false, false);
  return fmaxf(u2f(t[0]), u2f(t[1]));
}
__device__ __forceinline__ float row_sum(float x) { x += dpp<XOR8>(x); x += dpp<XOR7>(x); x += dpp<XOR2>(x); x += dpp<XOR1>(x); return x; }
__device__ __forceinline__ float row_max(float x) { x = fmaxf(x, dpp<XOR8>(x)); x = fmaxf(x, dpp<XOR7>(x)); x = fmaxf(x, dpp<XOR2>(x)); x = fmaxf(x, dpp<XOR1>(x)); return x; }
__device__ __forceinline__ float wave_sum(float x) { return xrow_sum(row_sum(x)); }
__device__ __forceinline__ float wave_max(float x) { return xrow_max(row_max(x)); }
__device__ __forceinline__ float dot8(const v4u k, const v4u q) {
  float d = dot2(k.x, q.x, 0.f); d = dot2(k.y, q.y, d); d = dot2(k.z, q.z, d); d = dot2(k.w, q.w, d); return d;
}
__device__ __forceinline__ float treduce16(float (&x)[16], int c) {
#define TR_STEP(HALF, CTRL, BIT) _Pragma("unroll") for (int h = 0; h < (HALF); ++h) { \
    const float lo_ = x[h], hi_ = x[h + (HALF)]; const float t1_ = lo_ + dpp<CTRL>(lo_), t2_ = hi_ + dpp<CTRL>(hi_); x[h] = (c & (BIT)) ? t2_ : t1_; }
  TR_STEP(8, XOR8, 8) TR_STEP(4, XOR7, 4) TR_STEP(2, XOR2, 2) TR_STEP(1, XOR1, 1)
#undef TR_STEP
  return x[0];
}
template <int T> __device__ __forceinline__ float bcast16(float p) { return dpp<0x150 + T>(p); }
__device__ __forceinline__ v4u ldnt(const GAS v4u* p) { return __builtin_nontemporal_load(p); }
__device__ __forceinline__ float sigmoidf_(float v) { return 1.0f / (1.0f + __expf(-v)); }

template <int T> __device__ __forceinline__ void pv_step(f32x2 (&acc)[4], const v4u v, float p) {
  const float pb = bcast16<T>(p);
  const f32x2 p2 = {pb, pb};
#pragma unroll
  for (int j = 0; j < 4; ++j) acc[j] = __builtin_elementwise_fma(p2, f32x2{bflo(v[j]), bfhi(v[j])}, acc[j]);
  asm volatile("" : "+v"(acc[0]), "+v"(acc[1]), "+v"(acc[2]), "+v"(acc[3]));
}
__device__ __forceinline__ v4u ldb_nt(__amdgpu_buffer_rsrc_t rs, int voff, int soff) { return __builtin_amdgcn_raw_buffer_load_b128(rs, voff, soff, 2); }
template <int T, int N> struct Unroll { template <class F> static __device__ __forceinline__ void run(F&& f) { f(std::integral_constant<int, T>{}); Unroll<T + 1, N>::run(f); } };
template <int N> struct Unroll<N, N> { template <class F> static __device__ __forceinline__ void run(F&&) {} };

__global__ void __launch_bounds__(NTHR) fused_kernel(const u16* __restrict__ xg, const u16* __restrict__ Kg, const u16* __restrict__ Vg, const u16* __restrict__ wq,
                                                     const u16* __restrict__ wo, const u16* __restrict__ w_r, const u16* __restrict__ w_gate, const u16* __restrict__ w_up,
                                                     const u16* __restrict__ w_down, const u16* __restrict__ gate_w, u16* __restrict__ out, unsigned char* __restrict__ ws) {
  extern __shared__ __attribute__((aligned(16))) unsigned char smem[];
  LAS unsigned char* L = (LAS unsigned char*)smem;
  const int tid = threadIdx.x, lane = tid & 63, w = __builtin_amdgcn_readfirstlane(tid >> 6);
  const int bid = blockIdx.x;
  const int h = (bid & 7) * 4 + (bid >> 6), b = (bid >> 3) & 7;
  const int r = lane >> 4, c = lane & 15;
  STAMP(0);

  if (w < NMAIN) {
    const GAS v4u* xp = (const GAS v4u*)(xg + b * DM + 64 * w + 16 * r);
    const v4u xa = xp[0], xb = xp[1];
    const GAS v4u* wqp = (const GAS v4u*)(wq + (size_t)(64 * w + 16 * r) * (NH * DH) + h * DH + 8 * c);
    v4u W[16];
#pragma unroll
    for (int t = 0; t < 16; ++t) W[t] = LDW(wqp + (size_t)t * (NH * DH / 8));
    const size_t kvoff = (size_t)(b * NH + h) * SK * DH;
    const __amdgpu_buffer_rsrc_t rsK = __builtin_amdgcn_make_buffer_rsrc((void*)(Kg + kvoff), 0, SK * DH * 2, 0x00020000);
    const __amdgpu_buffer_rsrc_t rsV = __builtin_amdgcn_make_buffer_rsrc((void*)(Vg + kvoff), 0, SK * DH * 2, 0x00020000);
    const int voff = w * 1024 + lane * 16;
    v4u P[16];
#pragma unroll
    for (int t = 0; t < 16; ++t) P[t] = ldb_nt(rsK, voff, t * 8192);
    __builtin_amdgcn_sched_barrier(0);
    float qa[8];
#pragma unroll
    for (int j = 0; j < 8; ++j) qa[j] = 0.f;
#pragma unroll
    for (int t = 0; t < 16; ++t) {
      const unsigned xw = (t < 8) ? xa[t >> 1] : xb[(t - 8) >> 1];
      const float xv = (t & 1) ? bfhi(xw) : bflo(xw);
#pragma unroll
      for (int j = 0; j < 4; ++j) { qa[2 * j] = fmaf(xv, bflo(W[t][j]), qa[2 * j]); qa[2 * j + 1] = fmaf(xv, bfhi(W[t][j]), qa[2 * j + 1]); }
    }
#pragma unroll
    for (int j = 0; j < 8; ++j) qa[j] = xrow_sum(qa[j]);
    if (r == 0) {
      LAS f32x4* qp = (LAS f32x4*)(L + L_QPART) + w * 32 + c * 2;
      qp[0] = f32x4{qa[0], qa[1], qa[2], qa[3]}; qp[1] = f32x4{qa[4], qa[5], qa[6], qa[7]};
    }
    __syncthreads();
    STAMP(1);
    v4u q;
    {
      f32x4 s0 = {0.f, 0.f, 0.f, 0.f}, s1 = {0.f, 0.f, 0.f, 0.f};
#pragma unroll
      for (int ww = 0; ww < 8; ++ww) { const LAS f32x4* qp = (const LAS f32x4*)(L + L_QPART) + ww * 32 + c * 2; s0 += qp[0]; s1 += qp[1]; }
      q = v4u{pack_bf16(s0[0], s0[1]), pack_bf16(s0[2], s0[3]), pack_bf16(s1[0], s1[1]), pack_bf16(s1[2], s1[3])};
    }
    const GAS v4u* wop = (const GAS v4u*)(wo + (size_t)(h * DH + 16 * w) * DM + lane * 8);
    v4u Wo[16];
#pragma unroll
    for (int t = 0; t < 16; ++t) Wo[t] = LDW(wop + t * (DM / 8));
    __builtin_amdgcn_sched_barrier(0);
    LAS float* sc = (LAS float*)(L + L_SC) + w * 512 + lane;
    v4u R[4];
    float x[16];
    float mx;
#pragma unroll
    for (int t = 0; t < 16; ++t) {
      x[t] = dot8(P[t], q);
      if (t >= 12) R[t - 12] = ldb_nt(rsK, voff, (16 + t - 12) * 8192);
      __builtin_amdgcn_sched_barrier(0);
    }
    { const float s = treduce16(x, c); sc[0] = s; mx = s; }
    for (int g = 1; g < 7; ++g) {
      const int so = (16 * g + 4) * 8192;
#pragma unroll
      for (int t = 0; t < 16; ++t) {
        x[t] = dot8(R[t & 3], q);
        R[t & 3] = ldb_nt(rsK, voff, so + t * 8192);
        __builtin_amdgcn_sched_barrier(0);
      }
      const float s = treduce16(x, c); sc[g * 64] = s; mx = fmaxf(mx, s);
    }
    {
      const int so = (16 * 7 + 4) * 8192;
#pragma unroll
      for (int t = 0; t < 16; ++t) {
        x[t] = dot8(R[t & 3], q);
        R[t & 3] = (t < 12) ? ldb_nt(rsK, voff, so + t * 8192) : ldb_nt(rsV, voff, (t - 12) * 8192);
        __builtin_amdgcn_sched_barrier(0);
      }
      const float s = treduce16(x, c); sc[7 * 64] = s; mx = fmaxf(mx, s);
    }
    STAMP(2);
    const float m = wave_max(mx);
    unsigned Wp[8][8];
#pragma unroll
    for (int i = 0; i < 8; ++i)
#pragma unroll
      for (int j = 0; j < 4; ++j) {
        Wp[i][2 * j] = __builtin_amdgcn_perm(Wo[2 * i + 1][j], Wo[2 * i][j], 0x05040100u);
        Wp[i][2 * j + 1] = __builtin_amdgcn_perm(Wo[2 * i + 1][j], Wo[2 * i][j], 0x07060302u);
        asm volatile("" : "+v"(Wp[i][2 * j]), "+v"(Wp[i][2 * j + 1]));
      }
    f32x2 acc2[4];
#pragma unroll
    for (int j = 0; j < 4; ++j) acc2[j] = f32x2{0.f, 0.f};
    float lsum = 0.f;
    const float mc = m * SM_C;
    for (int g = 0; g < 7; ++g) {
      const float p = __builtin_amdgcn_exp2f(fmaf(sc[g * 64], SM_C, -mc));
      lsum += p;
      const int so = (16 * g + 4) * 8192;
      Unroll<0, 16>::run([&](auto T) {
        constexpr int t = decltype(T)::value;
        pv_step<t>(acc2, R[t & 3], p);
        R[t & 3] = ldb_nt(rsV, voff, so + t * 8192);
        __builtin_amdgcn_sched_barrier(0);
      });
    }
    {
      const float p = __builtin_amdgcn_exp2f(fmaf(sc[7 * 64], SM_C, -mc));
      lsum += p;
      const int so = (16 * 7 + 4) * 8192;
      Unroll<0, 16>::run([&](auto T) {
        constexpr int t = decltype(T)::value;
        pv_step<t>(acc2, R[t & 3], p);
        if (t < 12) R[t & 3] = ldb_nt(rsV, voff, so + t * 8192);
        __builtin_amdgcn_sched_barrier(0);
      });
    }
    STAMP(3);
    float acc[8];
#pragma unroll
    for (int j = 0; j < 8; ++j) acc[j] = xrow_sum(acc2[j >> 1][j & 1]);
    lsum = wave_sum(lsum);
    if (r == 0) {
      LAS f32x4* pp = (LAS f32x4*)(L + L_PART) + w * 32 + c * 2;
      pp[0] = f32x4{acc[0], acc[1], acc[2], acc[3]}; pp[1] = f32x4{acc[4], acc[5], acc[6], acc[7]};
    }
    if (lane == 0) { LAS float* ml = (LAS float*)(L + L_ML); ml[2 * w] = m; ml[2 * w + 1] = lsum; }
    __syncthreads();
    STAMP(4);
    {
      const LAS float* ml = (const LAS float*)(L + L_ML);
      float M = ml[0];
#pragma unroll
      for (int ww = 1; ww < 8; ++ww) M = fmaxf(M, ml[2 * ww]);
      float Lsum = 0.f;
#pragma unroll
      for (int ww = 0; ww < 8; ++ww) Lsum += __builtin_amdgcn_exp2f((ml[2 * ww] - M) * SM_C) * ml[2 * ww + 1];
      const LAS float* part = (const LAS float*)(L + L_PART);
      const float e0 = __builtin_amdgcn_exp2f((ml[4 * r] - M) * SM_C), e1 = __builtin_amdgcn_exp2f((ml[4 * r + 2] - M) * SM_C);
      float o = e0 * part[(2 * r) * 128 + 16 * w + c] + e1 * part[(2 * r + 1) * 128 + 16 * w + c];
      o = xrow_sum(o) / Lsum;
      const unsigned op = pack_bf16(o, dpp<XOR1>(o));
      float y[8];
#pragma unroll
      for (int j = 0; j < 8; ++j) y[j] = 0.f;
#pragma unroll
      for (int i = 0; i < 8; ++i) {
        const unsigned os = __builtin_amdgcn_readlane(op, 2 * i);
#pragma unroll
        for (int j = 0; j < 8; ++j) y[j] = dot2(Wp[i][j], os, y[j]);
      }
      const float g = ((const LAS float*)(L + L_SCAL))[0];
      LAS f32x4* yp = (LAS f32x4*)(L + L_YP) + w * 128 + lane * 2;
      yp[0] = f32x4{y[0] * g, y[1] * g, y[2] * g, y[3] * g}; yp[1] = f32x4{y[4] * g, y[5] * g, y[6] * g, y[7] * g};
    }
  } else {
    const GAS v4u* xp = (const GAS v4u*)(xg + b * DM + 8 * lane);
    const v4u xv = xp[0];
    const v4u gw = *(const GAS v4u*)(gate_w + 8 * lane);
    const v4u wr = *(const GAS v4u*)(w_r + 8 * lane);
    const int rr = lane >> 3, cc = lane & 7, ccl = cc < 7 ? cc : 6;
    const GAS v4u* gp = (const GAS v4u*)(w_gate + (size_t)(64 * rr) * FF + h * FS + 8 * ccl);
    const GAS v4u* up = (const GAS v4u*)(w_up + (size_t)(64 * rr) * FF + h * FS + 8 * ccl);
    v4u G[8], U[8];
#pragma unroll
    for (int t = 0; t < 8; ++t) { G[t] = LDW(gp + t * (FF / 8)); U[t] = LDW(up + t * (FF / 8)); }
    __builtin_amdgcn_sched_barrier(0);
    __syncthreads();
    LAS float* xs = (LAS float*)(L + L_XS);
    { LAS f32x4* xs4 = (LAS f32x4*)xs + 2 * lane;
      xs4[0] = f32x4{bflo(xv.x), bfhi(xv.x), bflo(xv.y), bfhi(xv.y)}; xs4[1] = f32x4{bflo(xv.z), bfhi(xv.z), bflo(xv.w), bfhi(xv.w)}; }
    float sg = 0.f, sr = 0.f;
#pragma unroll
    for (int j = 0; j < 4; ++j) {
      sg += rbf(bflo(xv[j]) * bflo(gw[j])); sg += rbf(bfhi(xv[j]) * bfhi(gw[j]));
      sr = fmaf(bflo(xv[j]), bflo(wr[j]), sr); sr = fmaf(bfhi(xv[j]), bfhi(wr[j]), sr);
    }
    sg = rbf(wave_sum(sg)); sr = rbf(wave_sum(sr));
    const float g = rbf(sigmoidf_(sg)), rs = rbf(sigmoidf_(sr)), omg = rbf(1.0f - g);
    if (lane == 0) { LAS float* sca = (LAS float*)(L + L_SCAL); sca[0] = g; sca[1] = omg; }
    float ag[8], au[8];
#pragma unroll
    for (int j = 0; j < 8; ++j) { ag[j] = 0.f; au[j] = 0.f; }
    for (int it = 0; it < MOE_IT1; ++it) {
      v4u Gc[8], Uc[8];
#pragma unroll
      for (int t = 0; t < 8; ++t) { Gc[t] = G[t]; Uc[t] = U[t]; }
      if (it < 7) {
#pragma unroll
        for (int t = 0; t < 8; ++t) { G[t] = LDW(gp + ((it + 1) * 8 + t) * (FF / 8)); U[t] = LDW(up + ((it + 1) * 8 + t) * (FF / 8)); }
      }
      const f32x4 xq0 = *(const LAS f32x4*)(xs + 64 * rr + 8 * it), xq1 = *(const LAS f32x4*)(xs + 64 * rr + 8 * it + 4);
#pragma unroll
      for (int t = 0; t < 8; ++t) {
        const float xt = t < 4 ? xq0[t & 3] : xq1[t & 3];
#pragma unroll
        for (int j = 0; j < 4; ++j) {
          ag[2 * j] = fmaf(xt, bflo(Gc[t][j]), ag[2 * j]); ag[2 * j + 1] = fmaf(xt, bfhi(Gc[t][j]), ag[2 * j + 1]);
          au[2 * j] = fmaf(xt, bflo(Uc[t][j]), au[2 * j]); au[2 * j + 1] = fmaf(xt, bfhi(Uc[t][j]), au[2 * j + 1]);
        }
      }
    }
    const GAS v4u* dp = (const GAS v4u*)(w_down + (size_t)(h * FS) * DM + lane * 8);
    v4u Dn[14];
#pragma unroll
    for (int t = 0; t < 14; ++t) Dn[t] = LDW(dp + t * (DM / 8));
    LAS float* act = (LAS float*)(L + L_ACT);
#pragma unroll
    for (int j = 0; j < 8; ++j) {
      float gg = ag[j], uu = au[j];
      gg += dpp<XOR8>(gg); uu += dpp<XOR8>(uu);
      gg = rbf(xrow_sum(gg)); uu = rbf(xrow_sum(uu));
      const float si = rbf(gg * sigmoidf_(gg));
      if (lane < 7) act[8 * lane + j] = rbf(si * uu);
    }
    float y[8];
#pragma unroll
    for (int j = 0; j < 8; ++j) y[j] = 0.f;
    for (int it = 0; it < MOE_IT2; ++it) {
      v4u Dc[14];
#pragma unroll
      for (int t = 0; t < 14; ++t) Dc[t] = Dn[t];
      if (it < 3) {
#pragma unroll
        for (int t = 0; t < 14; ++t) Dn[t] = LDW(dp + ((it + 1) * 14 + t) * (DM / 8));
      }
#pragma unroll
      for (int t = 0; t < 14; ++t) {
        const float a = act[it * 14 + t];
#pragma unroll
        for (int j = 0; j < 4; ++j) { y[2 * j] = fmaf(a, bflo(Dc[t][j]), y[2 * j]); y[2 * j + 1] = fmaf(a, bfhi(Dc[t][j]), y[2 * j + 1]); }
      }
    }
    const float f = omg * rs;
    LAS f32x4* ym = (LAS f32x4*)(L + L_YMOE) + 2 * lane;
    ym[0] = f32x4{y[0] * f, y[1] * f, y[2] * f, y[3] * f}; ym[1] = f32x4{y[4] * f, y[5] * f, y[6] * f, y[7] * f};
    STAMP(9);
    __syncthreads();
  }
  __syncthreads();
  STAMP(5);
  const __amdgpu_buffer_rsrc_t rs_part = __builtin_amdgcn_make_buffer_rsrc((void*)(ws + WS_PART), 0, NB * NH * DM * 4, 0x00020000);
  if (tid < 128) {
    f32x4 s = ((const LAS f32x4*)(L + L_YMOE))[tid];
#pragma unroll
    for (int ww = 0; ww < 8; ++ww) s += ((const LAS f32x4*)(L + L_YP))[ww * 128 + tid];
    __builtin_amdgcn_raw_buffer_store_b128(__builtin_bit_cast(v4u, s), rs_part, ((b * NH + h) * DM + tid * 4) * 4, 0, 16);
    asm volatile("s_waitcnt vmcnt(0)" ::: "memory");
  }
  __syncthreads();
  STAMP(6);
  volatile LAS unsigned* flag = (volatile LAS unsigned*)(L + L_SCAL + 16);
  if (tid == 0) {
    const unsigned old = __hip_atomic_fetch_add((GAS unsigned*)(ws + WS_CNT) + b * 32, 1u, __ATOMIC_RELAXED, __HIP_MEMORY_SCOPE_AGENT);
    __builtin_amdgcn_fence(__ATOMIC_ACQUIRE, "wavefront");
    *flag = (old == NH - 1) ? 1u : 0u;
  }
  __syncthreads();
  STAMP(7);
  if (*flag == 0u) return;
  if (tid < 512) {
    const int cg = tid & 127, pg = tid >> 7;
    v4u t[8];
#pragma unroll
    for (int i = 0; i < 8; ++i) t[i] = __builtin_amdgcn_raw_buffer_load_b128(rs_part, ((b * NH + pg * 8 + i) * DM + cg * 4) * 4, 0, 16);
    f32x4 s = {0.f, 0.f, 0.f, 0.f};
#pragma unroll
    for (int i = 0; i < 8; ++i) s += __builtin_bit_cast(f32x4, t[i]);
    ((LAS f32x4*)(L + L_RED))[pg * 128 + cg] = s;
  }
  __syncthreads();
  if (tid < 128) {
    f32x4 s = ((const LAS f32x4*)(L + L_RED))[tid];
#pragma unroll
    for (int i = 1; i < 4; ++i) s += ((const LAS f32x4*)(L + L_RED))[i * 128 + tid];
    const float omg = ((const LAS float*)(L + L_SCAL))[1];
    const v2u xv = *(const GAS v2u*)(xg + b * DM + tid * 4);
    v2u o;
    o.x = pack_bf16(fmaf(omg, bflo(xv.x), s[0]), fmaf(omg, bfhi(xv.x), s[1]));
    o.y = pack_bf16(fmaf(omg, bflo(xv.y), s[2]), fmaf(omg, bfhi(xv.y), s[3]));
    *(GAS v2u*)(out + b * DM + tid * 4) = o;
  }
}
}

extern "C" void kernel_launch(void* const* d_in, const int* in_sizes, int n_in, void* d_out, int out_size, void* d_ws, size_t ws_size, hipStream_t stream) {
  (void)in_sizes; (void)n_in; (void)out_size; (void)ws_size;
  hipFuncSetAttribute((const void*)fused_kernel, hipFuncAttributeMaxDynamicSharedMemorySize, LDS_BYTES);
  hipMemsetAsync((unsigned char*)d_ws + WS_CNT, 0, WS_CNT_BYTES, stream);
  fused_kernel<<<dim3(NB * NH), dim3(NTHR), LDS_BYTES, stream>>>(
      (const u16*)d_in[0], (const u16*)d_in[1], (const u16*)d_in[2], (const u16*)d_in[3], (const u16*)d_in[4],
      (const u16*)d_in[6], (const u16*)d_in[7], (const u16*)d_in[8], (const u16*)d_in[9], (const u16*)d_in[10], (u16*)d_out, (unsigned char*)d_ws);
}
```

```cpp
#define INFL 24
#define W_M2 22
#define W_M1 24
#define W_P1 32
#define W_P2 44
#define TH1 10
#define TH2 28
#define NPEER 4
#define GOV_SLEEP 100
#include <hip/hip_runtime.h>
#include <stdint.h>
#include <type_traits>

#define GAS __attribute__((address_space(1)))
#define LAS __attribute__((address_space(3)))
typedef unsigned short u16;
typedef unsigned long long u64;
typedef unsigned v4u __attribute__((ext_vector_type(4)));
typedef unsigned v2u __attribute__((ext_vector_type(2)));
typedef float f32x4 __attribute__((ext_vector_type(4)));
typedef float f32x2 __attribute__((ext_vector_type(2)));
typedef __bf16 bf16x2 __attribute__((ext_vector_type(2)));

#define STAMP(i)
#define STAMP_OUT(i)
#define BLK 32
#define WQ_FIRST 4
#define WQ_STEP 4
#define MOE_SLEEP 32
#define BOOT 32
#define BOOT_END 256

namespace {
constexpr int NB = 8, NH = 32, SK = 4096, DH = 128, DM = 512, FF = 1792, FS = 64;
constexpr int NCONS = 8, NWAVES = NCONS + 3, NTHR = 64 * NWAVES;
constexpr float SM_C = 0.088388347648318440f * 1.4426950408889634f;
constexpr int NPIECE = 2048;
constexpr int RING = 128;
constexpr unsigned SPIN_CAP = 1u << 22;

constexpr int L_RING = 0;
constexpr int L_YP = 0;
constexpr int L_SC = RING * 1024;
constexpr int L_QPART = L_SC + 8 * 512 * 4;
constexpr int L_ML = L_QPART + 8 * 128 * 4;
constexpr int L_CTL = L_ML + 64;
constexpr int L_SCAL = L_CTL + 64;
constexpr int L_ACT = L_SCAL + 64;
constexpr int L_YMOE = L_ACT + 256;
constexpr int L_XS = L_YMOE + 512 * 4;
constexpr int LDS_BYTES = L_XS + 512 * 4;
static_assert(LDS_BYTES <= 160 * 1024, "lds");

constexpr size_t WS_EPOCH = 0;
constexpr size_t WS_READY = 4096;
constexpr size_t WS_PROG = 32768;
constexpr size_t WS_INBOX = 65536;

#define RLX_AGENT __ATOMIC_RELAXED, __HIP_MEMORY_SCOPE_AGENT

__device__ __forceinline__ float u2f(unsigned u) { return __builtin_bit_cast(float, u); }
__device__ __forceinline__ unsigned f2u(float f) { return __builtin_bit_cast(unsigned, f); }
__device__ __forceinline__ float bflo(unsigned u) { return u2f(u << 16); }
__device__ __forceinline__ float bfhi(unsigned u) { return u2f(u & 0xffff0000u); }
__device__ __forceinline__ float dot2(unsigned a, unsigned b, float acc) {
  return __builtin_amdgcn_fdot2_f32_bf16(__builtin_bit_cast(bf16x2, a), __builtin_bit_cast(bf16x2, b), acc, false);
}
__device__ __forceinline__ unsigned pack_bf16(float lo, float hi) {
  bf16x2 v = {(__bf16)lo, (__bf16)hi};
  return __builtin_bit_cast(unsigned, v);
}
__device__ __forceinline__ float rbf(float x) { return u2f((unsigned)__builtin_bit_cast(u16, (__bf16)x) << 16); }
template <int CTRL> __device__ __forceinline__ float dpp(float x) {
  return __builtin_bit_cast(float, __builtin_amdgcn_mov_dpp(__builtin_bit_cast(int, x), CTRL, 0xf, 0xf, true));
}
constexpr int XOR1 = 0xB1, XOR2 = 0x4E, XOR7 = 0x141, XOR8 = 0x128;
__device__ __forceinline__ float xrow_sum(float x) {
  auto s = __builtin_amdgcn_permlane16_swap(f2u(x), f2u(x), false, false);
  x = u2f(s[0]) + u2f(s[1]);
  auto t = __builtin_amdgcn_permlane32_swap(f2u(x), f2u(x), false, false);
  return u2f(t[0]) + u2f(t[1]);
}
__device__ __forceinline__ float xrow_max(float x) {
  auto s = __builtin_amdgcn_permlane16_swap(f2u(x), f2u(x), false, false);
  x = fmaxf(u2f(s[0]), u2f(s[1]));
  auto t = __builtin_amdgcn_permlane32_swap(f2u(x), f2u(x), false, false);
  return fmaxf(u2f(t[0]), u2f(t[1]));
}
__device__ __forceinline__ float row_sum(float x) { x += dpp<XOR8>(x); x += dpp<XOR7>(x); x += dpp<XOR2>(x); x += dpp<XOR1>(x); return x; }
__device__ __forceinline__ float row_max(float x) { x = fmaxf(x, dpp<XOR8>(x)); x = fmaxf(x, dpp<XOR7>(x)); x = fmaxf(x, dpp<XOR2>(x)); x = fmaxf(x, dpp<XOR1>(x)); return x; }
__device__ __forceinline__ float wave_sum(float x) { return xrow_sum(row_sum(x)); }
__device__ __forceinline__ float wave_max(float x) { return xrow_max(row_max(x)); }
__device__ __forceinline__ float dot8(const v4u k, const v4u q) {
  float d = dot2(k.x, q.x, 0.f); d = dot2(k.y, q.y, d); d = dot2(k.z, q.z, d); d = dot2(k.w, q.w, d); return d;
}
__device__ __forceinline__ float treduce16(float (&x)[16], int c) {
#define TR_STEP(HALF, CTRL, BIT) _Pragma("unroll") for (int h = 0; h < (HALF); ++h) { \
    const float lo_ = x[h], hi_ = x[h + (HALF)]; const float t1_ = lo_ + dpp<CTRL>(lo_), t2_ = hi_ + dpp<CTRL>(hi_); x[h] = (c & (BIT)) ? t2_ : t1_; }
  TR_STEP(8, XOR8, 8) TR_STEP(4, XOR7, 4) TR_STEP(2, XOR2, 2) TR_STEP(1, XOR1, 1)
#undef TR_STEP
  return x[0];
}
__device__ __forceinline__ float sigmoidf_(float v) { return 1.0f / (1.0f + __expf(-v)); }
template <int T> __device__ __forceinline__ void pv_step(f32x2 (&acc)[4], const v4u v, float p) {
  const float pb = dpp<0x150 + T>(p);
  const f32x2 p2 = {pb, pb};
#pragma unroll
  for (int j = 0; j < 4; ++j) acc[j] = __builtin_elementwise_fma(p2, f32x2{bflo(v[j]), bfhi(v[j])}, acc[j]);
}
template <int T, int N> struct Unroll { template <class F> static __device__ __forceinline__ void run(F&& f) { f(std::integral_constant<int, T>{}); Unroll<T + 1, N>::run(f); } };
template <int N> struct Unroll<N, N> { template <class F> static __device__ __forceinline__ void run(F&&) {} };
template <int N> __device__ __forceinline__ void wait_vm() { asm volatile("s_waitcnt vmcnt(%0)" ::"n"(N) : "memory"); }
__device__ __forceinline__ void wait_lgkm() { asm volatile("s_waitcnt lgkmcnt(0)" ::: "memory"); }
__device__ __forceinline__ void raw_barrier() { asm volatile("s_waitcnt lgkmcnt(0)\n\ts_barrier" ::: "memory"); }

__device__ __forceinline__ void wait_landed(volatile LAS unsigned* ctl, unsigned need, unsigned& have) {
  if (have >= need) return;
  for (unsigned spins = 0; spins < SPIN_CAP; ++spins) {
    have = __builtin_amdgcn_readfirstlane(ctl[0]);
    if (have >= need) break;
    __builtin_amdgcn_s_sleep(1);
  }
  asm volatile("" ::: "memory");
}

__device__ __forceinline__ int min_consumed(volatile LAS unsigned* ctl, int lane) {
  unsigned cn = ctl[8 + (lane & 7)];
  cn = min(cn, (unsigned)__builtin_amdgcn_mov_dpp((int)cn, XOR1, 0xf, 0xf, true));
  cn = min(cn, (unsigned)__builtin_amdgcn_mov_dpp((int)cn, XOR2, 0xf, 0xf, true));
  cn = min(cn, (unsigned)__builtin_amdgcn_mov_dpp((int)cn, XOR7, 0xf, 0xf, true));
  return (int)__builtin_amdgcn_readfirstlane(cn);
}

__global__ void __launch_bounds__(NTHR) fused_kernel(const u16* __restrict__ xg, const u16* __restrict__ Kg, const u16* __restrict__ Vg, const u16* __restrict__ wq,
                                                     const u16* __restrict__ wo, const u16* __restrict__ w_r, const u16* __restrict__ w_gate, const u16* __restrict__ w_up,
                                                     const u16* __restrict__ w_down, const u16* __restrict__ gate_w, u16* __restrict__ out, unsigned char* __restrict__ ws) {
  extern __shared__ __attribute__((aligned(16))) unsigned char smem[];
  LAS unsigned char* L = (LAS unsigned char*)smem;
  const int tid = threadIdx.x, lane = tid & 63, w = __builtin_amdgcn_readfirstlane(tid >> 6);
  const int bid = blockIdx.x;
  const int h = (bid & 7) * 4 + (bid >> 6), b = (bid >> 3) & 7;
  const int r = lane >> 4, c = lane & 15;
  volatile LAS unsigned* ctl = (volatile LAS unsigned*)(L + L_CTL);
  STAMP(0);

  if (w == 0) {
    __builtin_amdgcn_s_setprio(3);
    const size_t kvoff = (size_t)(b * NH + h) * SK * DH;
    const __amdgpu_buffer_rsrc_t rsK = __builtin_amdgcn_make_buffer_rsrc((void*)(Kg + kvoff), 0, SK * DH * 2, 0x00020000);
    const __amdgpu_buffer_rsrc_t rsV = __builtin_amdgcn_make_buffer_rsrc((void*)(Vg + kvoff), 0, SK * DH * 2, 0x00020000);
    const int voff = lane * 16;
    if (lane < 16) ctl[lane] = 0u;
#pragma unroll
    for (int i = 0; i < INFL; ++i) __builtin_amdgcn_raw_ptr_buffer_load_lds(rsK, (LAS void*)(L + L_RING + i * 1024), 16, voff, i * 1024, 0, 2);
    raw_barrier();
    int free_until = RING;
    auto make_room = [&](int gp) {
      if (gp + 4 > free_until) {
        free_until = 8 * min_consumed(ctl, lane) + RING;
        if (gp + 4 > free_until) {
          wait_vm<0>();
          ctl[0] = (unsigned)gp;
          for (unsigned spins = 0; spins < SPIN_CAP; ++spins) {
            free_until = 8 * min_consumed(ctl, lane) + RING;
            if (gp + 4 <= free_until) break;
            __builtin_amdgcn_s_sleep(1);
          }
        }
        asm volatile("" ::: "memory");
      }
    };
    for (int p = INFL; p < BOOT_END; p += 4) {
      make_room(p);
      LAS unsigned char* dst = L + L_RING + (p & (RING - 1)) * 1024;
#pragma unroll
      for (int i = 0; i < 4; ++i) __builtin_amdgcn_raw_ptr_buffer_load_lds(rsK, (LAS void*)(dst + i * 1024), 16, voff, (p + i) * 1024, 0, 2);
      wait_vm<BOOT>();
      if (p + 4 > BOOT) ctl[0] = (unsigned)(p + 4 - BOOT);
    }
    int pub = BOOT_END - BOOT;
    auto run_block = [&](auto WW, const __amdgpu_buffer_rsrc_t rs, int goff, int p0, int p1) {
      constexpr int W = decltype(WW)::value;
#pragma unroll 1
      for (int p = p0; p < p1; p += 4) {
        const int gp = goff + p;
        make_room(gp);
        LAS unsigned char* dst = L + L_RING + (gp & (RING - 1)) * 1024;
#pragma unroll
        for (int i = 0; i < 4; ++i) __builtin_amdgcn_raw_ptr_buffer_load_lds(rs, (LAS void*)(dst + i * 1024), 16, voff, (p + i) * 1024, 0, 2);
        wait_vm<W>();
        pub = max(pub, gp + 4 - W);
        ctl[0] = (unsigned)pub;
      }
    };
    for (int half = 0; half < 2; ++half) {
      const __amdgpu_buffer_rsrc_t rs = half ? rsV : rsK;
#pragma unroll 1
      for (int p = half ? 0 : BOOT_END; p < 1024; p += BLK) {
        const int cl = __builtin_amdgcn_readfirstlane(ctl[2]);
        if (cl == 0) run_block(std::integral_constant<int, INFL>{}, rs, half * 1024, p, p + BLK);
        else if (cl == 1) run_block(std::integral_constant<int, W_M2>{}, rs, half * 1024, p, p + BLK);
        else if (cl == 2) run_block(std::integral_constant<int, W_M1>{}, rs, half * 1024, p, p + BLK);
        else if (cl == 3) run_block(std::integral_constant<int, W_P1>{}, rs, half * 1024, p, p + BLK);
        else run_block(std::integral_constant<int, W_P2>{}, rs, half * 1024, p, p + BLK);
      }
      if (half == 0) { STAMP(2); }
    }
    wait_vm<0>();
    ctl[0] = (unsigned)NPIECE;
    STAMP(3);
    STAMP_OUT(0); STAMP_OUT(2); STAMP_OUT(3);
    raw_barrier();
  } else if (w <= NCONS) {
    const int cw = w - 1;
    asm volatile("s_barrier" ::: "memory");
    const GAS v4u* xp = (const GAS v4u*)(xg + b * DM + 64 * cw + 16 * r);
    const v4u xa = xp[0], xb = xp[1];
    const GAS v4u* wqp = (const GAS v4u*)(wq + (size_t)(64 * cw + 16 * r) * (NH * DH) + h * DH + 8 * c);
    v4u W[16];
    STAMP(6);
    const LAS v4u* rp = (const LAS v4u*)(L + L_RING + cw * 1024) + lane;
    unsigned have = 0;
    v4u P[16];
#pragma unroll
    for (int t = 0; t < WQ_FIRST; ++t) W[t] = wqp[(size_t)t * (NH * DH / 8)];
    __builtin_amdgcn_sched_barrier(0);
#pragma unroll
    for (int sub = 0; sub < 4; ++sub) {
      wait_landed(ctl, (unsigned)(8 * (4 * sub + 3) + cw + 1), have);
#pragma unroll
      for (int i = 0; i < 4; ++i) P[4 * sub + i] = rp[(4 * sub + i) * 512];
      wait_lgkm();
      asm volatile("" : "+v"(P[4 * sub]), "+v"(P[4 * sub + 1]), "+v"(P[4 * sub + 2]), "+v"(P[4 * sub + 3]));
      ctl[8 + cw] = (unsigned)(4 * sub + 4);
#pragma unroll
      for (int t = WQ_FIRST + sub * WQ_STEP; t < WQ_FIRST + (sub + 1) * WQ_STEP && t < 16; ++t) W[t] = wqp[(size_t)t * (NH * DH / 8)];
      __builtin_amdgcn_sched_barrier(0);
    }
    STAMP(7);
    __builtin_amdgcn_sched_barrier(0);
    float qa[8];
#pragma unroll
    for (int j = 0; j < 8; ++j) qa[j] = 0.f;
#pragma unroll
    for (int t = 0; t < 16; ++t) {
      const unsigned xw = (t < 8) ? xa[t >> 1] : xb[(t - 8) >> 1];
      const float xv = (t & 1) ? bfhi(xw) : bflo(xw);
#pragma unroll
      for (int j = 0; j < 4; ++j) { qa[2 * j] = fmaf(xv, bflo(W[t][j]), qa[2 * j]); qa[2 * j + 1] = fmaf(xv, bfhi(W[t][j]), qa[2 * j + 1]); }
    }
#pragma unroll
    for (int j = 0; j < 8; ++j) qa[j] = xrow_sum(qa[j]);
    if (r == 0) {
      LAS f32x4* qp = (LAS f32x4*)(L + L_QPART) + cw * 32 + c * 2;
      qp[0] = f32x4{qa[0], qa[1], qa[2], qa[3]}; qp[1] = f32x4{qa[4], qa[5], qa[6], qa[7]};
    }
    wait_lgkm();
    STAMP(11);
    if (lane == 0) __hip_atomic_fetch_add((LAS unsigned*)(L + L_CTL) + 1, 1u, __ATOMIC_RELAXED, __HIP_MEMORY_SCOPE_WORKGROUP);
    for (unsigned spins = 0; spins < SPIN_CAP; ++spins) { if (__builtin_amdgcn_readfirstlane(ctl[1]) >= (unsigned)NCONS) break; }
    asm volatile("" ::: "memory");
    v4u q;
    {
      f32x4 s0 = {0.f, 0.f, 0.f, 0.f}, s1 = {0.f, 0.f, 0.f, 0.f};
#pragma unroll
      for (int ww = 0; ww < 8; ++ww) { const LAS f32x4* qp = (const LAS f32x4*)(L + L_QPART) + ww * 32 + c * 2; s0 += qp[0]; s1 += qp[1]; }
      q = v4u{pack_bf16(s0[0], s0[1]), pack_bf16(s0[2], s0[3]), pack_bf16(s1[0], s1[1]), pack_bf16(s1[2], s1[3])};
    }
    if (cw == 0) { STAMP(1); STAMP_OUT(1); STAMP_OUT(6); STAMP_OUT(7); STAMP_OUT(11); }
    LAS float* sc = (LAS float*)(L + L_SC) + cw * 512 + lane;
    float mx;
    {
      float x[16];
#pragma unroll
      for (int t = 0; t < 16; ++t) x[t] = dot8(P[t], q);
      const float s = treduce16(x, c); sc[0] = s; mx = s;
    }
    const GAS v4u* wop = (const GAS v4u*)(wo + (size_t)(h * DH + 16 * cw) * DM + lane * 8);
    auto kgroup = [&](int g) {
      float x[16];
#pragma unroll
      for (int sub = 0; sub < 4; ++sub) {
        wait_landed(ctl, (unsigned)(8 * (16 * g + 4 * sub + 3) + cw + 1), have);
        v4u d[4];
#pragma unroll
        for (int i = 0; i < 4; ++i) d[i] = rp[(4 * sub + i) * 512];
        wait_lgkm();
        asm volatile("" : "+v"(d[0]), "+v"(d[1]), "+v"(d[2]), "+v"(d[3]));
        ctl[8 + cw] = (unsigned)(16 * g + 4 * sub + 4);
#pragma unroll
        for (int i = 0; i < 4; ++i) x[4 * sub + i] = dot8(d[i], q);
      }
      const float s = treduce16(x, c); sc[g * 64] = s; mx = fmaxf(mx, s);
    };
    v4u Wo[16];
#pragma unroll 1
    for (int g = 1; g < 3; ++g) kgroup(g);
#pragma unroll
    for (int t = 0; t < 8; ++t) Wo[t] = wop[t * 64];
    __builtin_amdgcn_sched_barrier(0);
#pragma unroll 1
    for (int g = 3; g < 5; ++g) kgroup(g);
#pragma unroll
    for (int t = 8; t < 16; ++t) Wo[t] = wop[t * 64];
    __builtin_amdgcn_sched_barrier(0);
#pragma unroll 1
    for (int g = 5; g < 8; ++g) kgroup(g);
    const float m = wave_max(mx);
    unsigned Wp[8][8];
#pragma unroll
    for (int i = 0; i < 8; ++i)
#pragma unroll
      for (int j = 0; j < 4; ++j) {
        Wp[i][2 * j] = __builtin_amdgcn_perm(Wo[2 * i + 1][j], Wo[2 * i][j], 0x05040100u);
        Wp[i][2 * j + 1] = __builtin_amdgcn_perm(Wo[2 * i + 1][j], Wo[2 * i][j], 0x07060302u);
        asm volatile("" : "+v"(Wp[i][2 * j]), "+v"(Wp[i][2 * j + 1]));
      }
    f32x2 acc2[4];
#pragma unroll
    for (int j = 0; j < 4; ++j) acc2[j] = f32x2{0.f, 0.f};
    float lsum = 0.f;
    const float mc = m * SM_C;
    for (int g = 0; g < 8; ++g) {
      const float p = __builtin_amdgcn_exp2f(fmaf(sc[g * 64], SM_C, -mc));
      lsum += p;
      Unroll<0, 4>::run([&](auto S) {
        constexpr int sub = decltype(S)::value;
        wait_landed(ctl, (unsigned)(1024 + 8 * (16 * g + 4 * sub + 3) + cw + 1), have);
        v4u d[4];
#pragma unroll
        for (int i = 0; i < 4; ++i) d[i] = rp[(4 * sub + i) * 512];
        wait_lgkm();
        asm volatile("" : "+v"(d[0]), "+v"(d[1]), "+v"(d[2]), "+v"(d[3]));
        ctl[8 + cw] = (unsigned)(128 + 16 * g + 4 * sub + 4);
        pv_step<4 * sub + 0>(acc2, d[0], p); pv_step<4 * sub + 1>(acc2, d[1], p); pv_step<4 * sub + 2>(acc2, d[2], p); pv_step<4 * sub + 3>(acc2, d[3], p);
        asm volatile("" : "+v"(acc2[0]), "+v"(acc2[1]), "+v"(acc2[2]), "+v"(acc2[3]));
      });
    }
    float acc[8];
#pragma unroll
    for (int j = 0; j < 8; ++j) acc[j] = xrow_sum(acc2[j >> 1][j & 1]);
    lsum = wave_sum(lsum);
    if (r == 0) {
      LAS f32x4* pp = (LAS f32x4*)(L + L_QPART) + cw * 32 + c * 2;
      pp[0] = f32x4{acc[0], acc[1], acc[2], acc[3]}; pp[1] = f32x4{acc[4], acc[5], acc[6], acc[7]};
    }
    if (lane == 0) { LAS float* ml = (LAS float*)(L + L_ML); ml[2 * cw] = m; ml[2 * cw + 1] = lsum; }
    raw_barrier();
    if (cw == 0) { STAMP(4); STAMP_OUT(4); }
    {
      const LAS float* ml = (const LAS float*)(L + L_ML);
      float M = ml[0];
#pragma unroll
      for (int ww = 1; ww < 8; ++ww) M = fmaxf(M, ml[2 * ww]);
      float Lsum = 0.f;
#pragma unroll
      for (int ww = 0; ww < 8; ++ww) Lsum += __builtin_amdgcn_exp2f((ml[2 * ww] - M) * SM_C) * ml[2 * ww + 1];
      const LAS float* part = (const LAS float*)(L + L_QPART);
      const float e0 = __builtin_amdgcn_exp2f((ml[4 * r] - M) * SM_C), e1 = __builtin_amdgcn_exp2f((ml[4 * r + 2] - M) * SM_C);
      float o = e0 * part[(2 * r) * 128 + 16 * cw + c] + e1 * part[(2 * r + 1) * 128 + 16 * cw + c];
      o = xrow_sum(o) / Lsum;
      const unsigned op = pack_bf16(o, dpp<XOR1>(o));
      float y[8];
#pragma unroll
      for (int j = 0; j < 8; ++j) y[j] = 0.f;
#pragma unroll
      for (int i = 0; i < 8; ++i) {
        const unsigned os = __builtin_amdgcn_readlane(op, 2 * i);
#pragma unroll
        for (int j = 0; j < 8; ++j) y[j] = dot2(Wp[i][j], os, y[j]);
      }
      const float g = ((const LAS float*)(L + L_SCAL))[0];
      LAS f32x4* yp = (LAS f32x4*)(L + L_YP) + cw * 128 + lane * 2;
      yp[0] = f32x4{y[0] * g, y[1] * g, y[2] * g, y[3] * g}; yp[1] = f32x4{y[4] * g, y[5] * g, y[6] * g, y[7] * g};
    }
  } else if (w == NCONS + 2) {
    asm volatile("s_barrier" ::: "memory");
    const unsigned tagb = ((unsigned)__hip_atomic_load((GAS u64*)(ws + WS_EPOCH), RLX_AGENT) + 1u) & 0xffffu;
    GAS unsigned* board = (GAS unsigned*)(ws + WS_PROG);
    for (unsigned it = 0; it < 4096; ++it) {
      __builtin_amdgcn_s_sleep(GOV_SLEEP);
      const unsigned my = __builtin_amdgcn_readfirstlane(ctl[0]);
      if (my >= (unsigned)(NPIECE - 192)) break;
      const unsigned me = (my - (unsigned)((__builtin_amdgcn_s_memrealtime() * 287ull) >> 10)) & 0xffffu;
      if (lane == 0) __hip_atomic_store(board + bid * 32, (tagb << 16) | me, RLX_AGENT);
      float sum = 0.f, cnt = 0.f;
      if (lane < NPEER) {
        const unsigned ej = __hip_atomic_load(board + ((bid + 1 + 33 * lane + 7 * it) & 255) * 32, RLX_AGENT);
        const bool ok = (ej >> 16) == tagb; sum = ok ? (float)(short)(u16)(me - ej) : 0.f; cnt = ok ? 1.f : 0.f;
      }
      sum = row_sum(sum); cnt = row_sum(cnt);
      const float diff = u2f(__builtin_amdgcn_readfirstlane(f2u(sum / fmaxf(cnt, 1.f))));
      cnt = u2f(__builtin_amdgcn_readfirstlane(f2u(cnt)));
      const unsigned cls = (cnt < (float)(NPEER / 2)) ? 0u : (diff >= (float)TH2) ? 1u : (diff >= (float)TH1) ? 2u : (diff <= (float)-TH2) ? 4u : (diff <= (float)-TH1) ? 3u : 0u;
      if (lane == 0) ctl[2] = cls;
    }
    raw_barrier();
  } else {
    asm volatile("s_barrier" ::: "memory");
    GAS u64* epoch = (GAS u64*)(ws + WS_EPOCH) + b * 16;
    const u64 e0 = __hip_atomic_load(epoch, RLX_AGENT);
    const GAS v4u* xp = (const GAS v4u*)(xg + b * DM + 8 * lane);
    const v4u xv = xp[0];
    const v4u gw = *(const GAS v4u*)(gate_w + 8 * lane);
    const v4u wr = *(const GAS v4u*)(w_r + 8 * lane);
    const u64 tag64 = e0 + 1ull;
    const unsigned tag32 = (unsigned)tag64 ^ (unsigned)(tag64 >> 32);
    {
      const __amdgpu_buffer_rsrc_t rsI = __builtin_amdgcn_make_buffer_rsrc((void*)(ws + WS_INBOX + (size_t)(b * NH + h) * 4096), 0, 4096, 0x00020000);
      const v4u inv = {0u, ~tag32, 0u, ~tag32};
#pragma unroll
      for (int i = 0; i < 4; ++i) __builtin_amdgcn_raw_buffer_store_b128(inv, rsI, lane * 16 + i * 1024, 0, 16);
      wait_vm<0>();
      if (lane == 0) __hip_atomic_store((GAS u64*)(ws + WS_READY) + b * NH + h, tag64, RLX_AGENT);
    }
    LAS float* xs = (LAS float*)(L + L_XS);
    { LAS f32x4* xs4 = (LAS f32x4*)xs + 2 * lane;
      xs4[0] = f32x4{bflo(xv.x), bfhi(xv.x), bflo(xv.y), bfhi(xv.y)}; xs4[1] = f32x4{bflo(xv.z), bfhi(xv.z), bflo(xv.w), bfhi(xv.w)}; }
    float sg = 0.f, sr = 0.f;
#pragma unroll
    for (int j = 0; j < 4; ++j) {
      sg += rbf(bflo(xv[j]) * bflo(gw[j])); sg += rbf(bfhi(xv[j]) * bfhi(gw[j]));
      sr = fmaf(bflo(xv[j]), bflo(wr[j]), sr); sr = fmaf(bfhi(xv[j]), bfhi(wr[j]), sr);
    }
    sg = rbf(wave_sum(sg)); sr = rbf(wave_sum(sr));
    const float g = rbf(sigmoidf_(sg)), rs = rbf(sigmoidf_(sr)), omg = rbf(1.0f - g);
    if (lane == 0) { LAS float* sca = (LAS float*)(L + L_SCAL); sca[0] = g; sca[1] = omg; sca[2] = u2f(tag32); ((LAS u64*)sca)[2] = tag64; }
    float y[8];
#pragma unroll
    for (int j = 0; j < 8; ++j) y[j] = 0.f;
    if (h < FF / FS) {
      for (unsigned spins = 0; spins < SPIN_CAP; ++spins) { if (__builtin_amdgcn_readfirstlane(ctl[1]) >= (unsigned)NCONS) break; __builtin_amdgcn_s_sleep(8); }
      const int rr = lane >> 3, cc = lane & 7;
      const GAS v4u* gp = (const GAS v4u*)(w_gate + (size_t)(64 * rr) * FF + h * FS + 8 * cc);
      const GAS v4u* up = (const GAS v4u*)(w_up + (size_t)(64 * rr) * FF + h * FS + 8 * cc);
      float ag[8], au[8];
#pragma unroll
      for (int j = 0; j < 8; ++j) { ag[j] = 0.f; au[j] = 0.f; }
#pragma unroll 1
      for (int it = 0; it < 16; ++it) {
        v4u Gc[4], Uc[4];
#pragma unroll
        for (int t = 0; t < 4; ++t) { Gc[t] = gp[(it * 4 + t) * (FF / 8)]; Uc[t] = up[(it * 4 + t) * (FF / 8)]; }
        const f32x4 xq = *(const LAS f32x4*)(xs + 64 * rr + 4 * it);
#pragma unroll
        for (int t = 0; t < 4; ++t) {
          const float xt = xq[t];
#pragma unroll
          for (int j = 0; j < 4; ++j) {
            ag[2 * j] = fmaf(xt, bflo(Gc[t][j]), ag[2 * j]); ag[2 * j + 1] = fmaf(xt, bfhi(Gc[t][j]), ag[2 * j + 1]);
            au[2 * j] = fmaf(xt, bflo(Uc[t][j]), au[2 * j]); au[2 * j + 1] = fmaf(xt, bfhi(Uc[t][j]), au[2 * j + 1]);
          }
        }
        __builtin_amdgcn_s_sleep(MOE_SLEEP);
      }
      LAS float* act = (LAS float*)(L + L_ACT);
#pragma unroll
      for (int j = 0; j < 8; ++j) {
        float gg = ag[j], uu = au[j];
        gg += dpp<XOR8>(gg); uu += dpp<XOR8>(uu);
        gg = rbf(xrow_sum(gg)); uu = rbf(xrow_sum(uu));
        const float si = rbf(gg * sigmoidf_(gg));
        if (lane < 8) act[8 * lane + j] = rbf(si * uu);
      }
      const GAS v4u* dp = (const GAS v4u*)(w_down + (size_t)(h * FS) * DM + lane * 8);
#pragma unroll 1
      for (int it = 0; it < 8; ++it) {
        v4u Dc[8];
#pragma unroll
        for (int t = 0; t < 8; ++t) Dc[t] = dp[(it * 8 + t) * (DM / 8)];
#pragma unroll
        for (int t = 0; t < 8; ++t) {
          const float a = act[it * 8 + t];
#pragma unroll
          for (int j = 0; j < 4; ++j) { y[2 * j] = fmaf(a, bflo(Dc[t][j]), y[2 * j]); y[2 * j + 1] = fmaf(a, bfhi(Dc[t][j]), y[2 * j + 1]); }
        }
        __builtin_amdgcn_s_sleep(MOE_SLEEP);
      }
    }
    const float f = omg * rs;
    LAS f32x4* ym = (LAS f32x4*)(L + L_YMOE) + 2 * lane;
    ym[0] = f32x4{y[0] * f, y[1] * f, y[2] * f, y[3] * f}; ym[1] = f32x4{y[4] * f, y[5] * f, y[6] * f, y[7] * f};
    {
      GAS u64* rdy = (GAS u64*)(ws + WS_READY) + b * NH + (lane & 31);
      for (unsigned spins = 0; spins < SPIN_CAP; ++spins) {
        const u64 v = __hip_atomic_load(rdy, RLX_AGENT);
        if (__all(v == tag64)) break;
        __builtin_amdgcn_s_sleep(8);
      }
    }
    STAMP(9); STAMP_OUT(9);
    raw_barrier();
  }
  raw_barrier();
  if (w == 1) { STAMP(5); STAMP_OUT(5); }
  const unsigned tag32 = f2u(((const LAS float*)(L + L_SCAL))[2]);
  if (tid < 512) {
    float s = ((const LAS float*)(L + L_YMOE))[tid];
#pragma unroll
    for (int ww = 0; ww < 8; ++ww) s += ((const LAS float*)(L + L_YP))[ww * 512 + tid];
    GAS u64* dst = (GAS u64*)(ws + WS_INBOX) + ((size_t)(b * NH + (tid >> 4)) * NH + h) * 16 + (tid & 15);
    __hip_atomic_store(dst, ((u64)tag32 << 32) | f2u(s), RLX_AGENT);
  }
  if (w == 0) {
    GAS u64* src = (GAS u64*)(ws + WS_INBOX) + ((size_t)(b * NH + h) * NH + 8 * r) * 16 + c;
    const float xres = bflo((unsigned)xg[b * DM + 16 * h + c]);
    unsigned v[8];
    for (unsigned spins = 0; spins < SPIN_CAP; ++spins) {
      bool ok = true;
#pragma unroll
      for (int i = 0; i < 8; ++i) { const u64 x = __hip_atomic_load(src + i * 16, RLX_AGENT); v[i] = (unsigned)x; ok &= (unsigned)(x >> 32) == tag32; }
      if (__all(ok)) break;
    }
    float s = 0.f;
#pragma unroll
    for (int i = 0; i < 8; ++i) s += u2f(v[i]);
    s = xrow_sum(s);
    const float omg = ((const LAS float*)(L + L_SCAL))[1];
    s = fmaf(omg, xres, s);
    const unsigned o2 = pack_bf16(s, dpp<XOR1>(s));
    if (lane < 16 && (lane & 1) == 0) *(GAS unsigned*)(out + b * DM + 16 * h + lane) = o2;
    if (h == 0 && lane == 0) __hip_atomic_store((GAS u64*)(ws + WS_EPOCH) + b * 16, ((const LAS u64*)(L + L_SCAL))[2], RLX_AGENT);
  }
}
}

extern "C" void kernel_launch(void* const* d_in, const int* in_sizes, int n_in, void* d_out, int out_size, void* d_ws, size_t ws_size, hipStream_t stream) {
  (void)in_sizes; (void)n_in; (void)out_size; (void)ws_size;
  (void)hipFuncSetAttribute((const void*)fused_kernel, hipFuncAttributeMaxDynamicSharedMemorySize, LDS_BYTES);
  fused_kernel<<<dim3(NB * NH), dim3(NTHR), LDS_BYTES, stream>>>(
      (const u16*)d_in[0], (const u16*)d_in[1], (const u16*)d_in[2], (const u16*)d_in[3], (const u16*)d_in[4],
      (const u16*)d_in[6], (const u16*)d_in[7], (const u16*)d_in[8], (const u16*)d_in[9], (const u16*)d_in[10], (u16*)d_out, (unsigned char*)d_ws);
}
```

```cpp
#include <hip/hip_runtime.h>
#include <stdint.h>
#include <type_traits>

#define GAS __attribute__((address_space(1)))
#define LAS __attribute__((address_space(3)))
typedef unsigned short u16;
typedef unsigned long long u64;
typedef unsigned v4u __attribute__((ext_vector_type(4)));
typedef unsigned v2u __attribute__((ext_vector_type(2)));
typedef float f32x4 __attribute__((ext_vector_type(4)));
typedef float f32x2 __attribute__((ext_vector_type(2)));
typedef __bf16 bf16x2 __attribute__((ext_vector_type(2)));

#define STAMP(i)
#define STAMP_OUT(i)
#define INFL 24
#define W_M2 22
#define W_M1 24
#define W_P1 32
#define W_P2 44
#define TH1 10
#define TH2 28
#define BLK 32
#define POLL_STAG 4
#define NPEER 4
#define GOV_SLEEP 100
#define WQ_FIRST 4
#define WQ_STEP 4
#define MOE_SLEEP 32
#define BOOT 32
#define BOOT_END 256

namespace {
constexpr int NB = 8, NH = 32, SK = 4096, DH = 128, DM = 512, FF = 1792, FS = 64;
constexpr int NCONS = 8, NWAVES = NCONS + 3, NTHR = 64 * NWAVES;
constexpr float SM_C = 0.088388347648318440f * 1.4426950408889634f;
constexpr int NPIECE = 2048;
constexpr int RING = 128;
constexpr unsigned SPIN_CAP = 1u << 22;

constexpr int L_RING = 0;
constexpr int L_SC = RING * 1024;
constexpr int L_QPART = L_SC + 8 * 512 * 4;
constexpr int L_ML = L_QPART + 8 * 128 * 4;
constexpr int L_CTL = L_ML + 64;
constexpr int L_SCAL = L_CTL + 64;
constexpr int L_ACT = L_SCAL + 64;
constexpr int L_YMOE = L_ACT + 256;
constexpr int L_XS = L_YMOE + 512 * 4;
constexpr int LDS_BYTES = L_XS + 512 * 4;
static_assert(LDS_BYTES <= 160 * 1024, "lds");

constexpr size_t WS_EPOCH = 0;
constexpr size_t WS_READY = 4096;
constexpr size_t WS_PROG = 32768;
constexpr size_t WS_INBOX = 65536;

#define RLX_AGENT __ATOMIC_RELAXED, __HIP_MEMORY_SCOPE_AGENT

__device__ __forceinline__ float u2f(unsigned u) { return __builtin_bit_cast(float, u); }
__device__ __forceinline__ unsigned f2u(float f) { return __builtin_bit_cast(unsigned, f); }
__device__ __forceinline__ float bflo(unsigned u) { return u2f(u << 16); }
__device__ __forceinline__ float bfhi(unsigned u) { return u2f(u & 0xffff0000u); }
__device__ __forceinline__ float dot2(unsigned a, unsigned b, float acc) {
  return __builtin_amdgcn_fdot2_f32_bf16(__builtin_bit_cast(bf16x2, a), __builtin_bit_cast(bf16x2, b), acc, false);
}
__device__ __forceinline__ unsigned pack_bf16(float lo, float hi) {
  bf16x2 v = {(__bf16)lo, (__bf16)hi};
  return __builtin_bit_cast(unsigned, v);
}
__device__ __forceinline__ float rbf(float x) { return u2f((unsigned)__builtin_bit_cast(u16, (__bf16)x) << 16); }
template <int CTRL> __device__ __forceinline__ float dpp(float x) {
  return __builtin_bit_cast(float, __builtin_amdgcn_mov_dpp(__builtin_bit_cast(int, x), CTRL, 0xf, 0xf, true));
}
constexpr int XOR1 = 0xB1, XOR2 = 0x4E, XOR7 = 0x141, XOR8 = 0x128;
__device__ __forceinline__ float xrow_sum(float x) {
  auto s = __builtin_amdgcn_permlane16_swap(f2u(x), f2u(x), false, false);
  x = u2f(s[0]) + u2f(s[1]);
  auto t = __builtin_amdgcn_permlane32_swap(f2u(x), f2u(x), false, false);
  return u2f(t[0]) + u2f(t[1]);
}
__device__ __forceinline__ float xrow_max(float x) {
  auto s = __builtin_amdgcn_permlane16_swap(f2u(x), f2u(x), false, false);
  x = fmaxf(u2f(s[0]), u2f(s[1]));
  auto t = __builtin_amdgcn_permlane32_swap(f2u(x), f2u(x), false, false);
  return fmaxf(u2f(t[0]), u2f(t[1]));
}
__device__ __forceinline__ float row_sum(float x) { x += dpp<XOR8>(x); x += dpp<XOR7>(x); x += dpp<XOR2>(x); x += dpp<XOR1>(x); return x; }
__device__ __forceinline__ float row_max(float x) { x = fmaxf(x, dpp<XOR8>(x)); x = fmaxf(x, dpp<XOR7>(x)); x = fmaxf(x, dpp<XOR2>(x)); x = fmaxf(x, dpp<XOR1>(x)); return x; }
__device__ __forceinline__ float wave_sum(float x) { return xrow_sum(row_sum(x)); }
__device__ __forceinline__ float wave_max(float x) { return xrow_max(row_max(x)); }
__device__ __forceinline__ float dot8(const v4u k, const v4u q) {
  float d = dot2(k.x, q.x, 0.f); d = dot2(k.y, q.y, d); d = dot2(k.z, q.z, d); d = dot2(k.w, q.w, d); return d;
}
__device__ __forceinline__ float treduce16(float (&x)[16], int c) {
#define TR_STEP(HALF, CTRL, BIT) _Pragma("unroll") for (int h = 0; h < (HALF); ++h) { \
    const float lo_ = x[h], hi_ = x[h + (HALF)]; const float t1_ = lo_ + dpp<CTRL>(lo_), t2_ = hi_ + dpp<CTRL>(hi_); x[h] = (c & (BIT)) ? t2_ : t1_; }
  TR_STEP(8, XOR8, 8) TR_STEP(4, XOR7, 4) TR_STEP(2, XOR2, 2) TR_STEP(1, XOR1, 1)
#undef TR_STEP
  return x[0];
}
__device__ __forceinline__ float sigmoidf_(float v) { return 1.0f / (1.0f + __expf(-v)); }
template <int T> __device__ __forceinline__ void pv_step(f32x2 (&acc)[4], const v4u v, float p) {
  const float pb = dpp<0x150 + T>(p);
  const f32x2 p2 = {pb, pb};
#pragma unroll
  for (int j = 0; j < 4; ++j) acc[j] = __builtin_elementwise_fma(p2, f32x2{bflo(v[j]), bfhi(v[j])}, acc[j]);
}
template <int T, int N> struct Unroll { template <class F> static __device__ __forceinline__ void run(F&& f) { f(std::integral_constant<int, T>{}); Unroll<T + 1, N>::run(f); } };
template <int N> struct Unroll<N, N> { template <class F> static __device__ __forceinline__ void run(F&&) {} };
template <int N> __device__ __forceinline__ void wait_vm() { asm volatile("s_waitcnt vmcnt(%0)" ::"n"(N) : "memory"); }
__device__ __forceinline__ void wait_lgkm() { asm volatile("s_waitcnt lgkmcnt(0)" ::: "memory"); }
__device__ __forceinline__ void raw_barrier() { asm volatile("s_waitcnt lgkmcnt(0)\n\ts_barrier" ::: "memory"); }

__device__ __forceinline__ void wait_landed(volatile LAS unsigned* ctl, unsigned need, unsigned& have) {
  if (have >= need) return;
  for (unsigned spins = 0; spins < SPIN_CAP; ++spins) {
    have = __builtin_amdgcn_readfirstlane(ctl[0]);
    if (have >= need) break;
    __builtin_amdgcn_s_sleep(1);
  }
  asm volatile("" ::: "memory");
}

__device__ __forceinline__ int min_consumed(volatile LAS unsigned* ctl, int lane) {
  unsigned cn = ctl[8 + (lane & 7)];
  cn = min(cn, (unsigned)__builtin_amdgcn_mov_dpp((int)cn, XOR1, 0xf, 0xf, true));
  cn = min(cn, (unsigned)__builtin_amdgcn_mov_dpp((int)cn, XOR2, 0xf, 0xf, true));
  cn = min(cn, (unsigned)__builtin_amdgcn_mov_dpp((int)cn, XOR7, 0xf, 0xf, true));
  return (int)__builtin_amdgcn_readfirstlane(cn);
}

__global__ void __launch_bounds__(NTHR) fused_kernel(const u16* __restrict__ xg, const u16* __restrict__ Kg, const u16* __restrict__ Vg, const u16* __restrict__ wq,
                                                     const u16* __restrict__ wo, const u16* __restrict__ w_r, const u16* __restrict__ w_gate, const u16* __restrict__ w_up,
                                                     const u16* __restrict__ w_down, const u16* __restrict__ gate_w, u16* __restrict__ out, unsigned char* __restrict__ ws) {
  extern __shared__ __attribute__((aligned(16))) unsigned char smem[];
  LAS unsigned char* L = (LAS unsigned char*)smem;
  const int tid = threadIdx.x, lane = tid & 63, w = __builtin_amdgcn_readfirstlane(tid >> 6);
  const int bid = blockIdx.x;
  const int h = (bid & 7) * 4 + (bid >> 6), b = (bid >> 3) & 7;
  const int r = lane >> 4, c = lane & 15;
  volatile LAS unsigned* ctl = (volatile LAS unsigned*)(L + L_CTL);
  STAMP(0);

  if (w == 0) {
    __builtin_amdgcn_s_setprio(3);
    const size_t kvoff = (size_t)(b * NH + h) * SK * DH;
    const __amdgpu_buffer_rsrc_t rsK = __builtin_amdgcn_make_buffer_rsrc((void*)(Kg + kvoff), 0, SK * DH * 2, 0x00020000);
    const __amdgpu_buffer_rsrc_t rsV = __builtin_amdgcn_make_buffer_rsrc((void*)(Vg + kvoff), 0, SK * DH * 2, 0x00020000);
    const int voff = lane * 16;
    if (lane < 16) ctl[lane] = 0u;
#pragma unroll
    for (int i = 0; i < INFL; ++i) __builtin_amdgcn_raw_ptr_buffer_load_lds(rsK, (LAS void*)(L + L_RING + i * 1024), 16, voff, i * 1024, 0, 2);
    raw_barrier();
    int free_until = RING;
    auto make_room = [&](int gp) {
      if (gp + 4 > free_until) {
        free_until = 8 * min_consumed(ctl, lane) + RING;
        if (gp + 4 > free_until) {
          wait_vm<0>();
          ctl[0] = (unsigned)gp;
          for (unsigned spins = 0; spins < SPIN_CAP; ++spins) {
            free_until = 8 * min_consumed(ctl, lane) + RING;
            if (gp + 4 <= free_until) break;
            __builtin_amdgcn_s_sleep(1);
          }
        }
        asm volatile("" ::: "memory");
      }
    };
    for (int p = INFL; p < BOOT_END; p += 4) {
      make_room(p);
      LAS unsigned char* dst = L + L_RING + (p & (RING - 1)) * 1024;
#pragma unroll
      for (int i = 0; i < 4; ++i) __builtin_amdgcn_raw_ptr_buffer_load_lds(rsK, (LAS void*)(dst + i * 1024), 16, voff, (p + i) * 1024, 0, 2);
      wait_vm<BOOT>();
      if (p + 4 > BOOT) ctl[0] = (unsigned)(p + 4 - BOOT);
    }
    int pub = BOOT_END - BOOT;
    auto run_block = [&](auto WW, const __amdgpu_buffer_rsrc_t rs, int goff, int p0, int p1) {
      constexpr int W = decltype(WW)::value;
#pragma unroll 1
      for (int p = p0; p < p1; p += 4) {
        const int gp = goff + p;
        make_room(gp);
        LAS unsigned char* dst = L + L_RING + (gp & (RING - 1)) * 1024;
#pragma unroll
        for (int i = 0; i < 4; ++i) __builtin_amdgcn_raw_ptr_buffer_load_lds(rs, (LAS void*)(dst + i * 1024), 16, voff, (p + i) * 1024, 0, 2);
        wait_vm<W>();
        pub = max(pub, gp + 4 - W);
        ctl[0] = (unsigned)pub;
      }
    };
    for (int half = 0; half < 2; ++half) {
      const __amdgpu_buffer_rsrc_t rs = half ? rsV : rsK;
#pragma unroll 1
      for (int p = half ? 0 : BOOT_END; p < 1024; p += BLK) {
        const int cl = __builtin_amdgcn_readfirstlane(ctl[2]);
        if (cl == 0) run_block(std::integral_constant<int, INFL>{}, rs, half * 1024, p, p + BLK);
        else if (cl == 1) run_block(std::integral_constant<int, W_M2>{}, rs, half * 1024, p, p + BLK);
        else if (cl == 2) run_block(std::integral_constant<int, W_M1>{}, rs, half * 1024, p, p + BLK);
        else if (cl == 3) run_block(std::integral_constant<int, W_P1>{}, rs, half * 1024, p, p + BLK);
        else run_block(std::integral_constant<int, W_P2>{}, rs, half * 1024, p, p + BLK);
      }
      if (half == 0) { STAMP(2); }
    }
    wait_vm<0>();
    ctl[0] = (unsigned)NPIECE;
    STAMP(3);
    STAMP_OUT(0); STAMP_OUT(2); STAMP_OUT(3);
    raw_barrier();
  } else if (w <= NCONS) {
    const int cw = w - 1;
    asm volatile("s_barrier" ::: "memory");
    const GAS v4u* xp = (const GAS v4u*)(xg + b * DM + 64 * cw + 16 * r);
    const v4u xa = xp[0], xb = xp[1];
    const GAS v4u* wqp = (const GAS v4u*)(wq + (size_t)(64 * cw + 16 * r) * (NH * DH) + h * DH + 8 * c);
    v4u W[16];
    STAMP(6);
    const LAS v4u* rp = (const LAS v4u*)(L + L_RING + cw * 1024) + lane;
    unsigned have = 0;
    v4u P[16];
#pragma unroll
    for (int t = 0; t < WQ_FIRST; ++t) W[t] = wqp[(size_t)t * (NH * DH / 8)];
    __builtin_amdgcn_sched_barrier(0);
#pragma unroll
    for (int sub = 0; sub < 4; ++sub) {
      wait_landed(ctl, (unsigned)(8 * (4 * sub + 3) + cw + 1), have);
#pragma unroll
      for (int i = 0; i < 4; ++i) P[4 * sub + i] = rp[(4 * sub + i) * 512];
      wait_lgkm();
      asm volatile("" : "+v"(P[4 * sub]), "+v"(P[4 * sub + 1]), "+v"(P[4 * sub + 2]), "+v"(P[4 * sub + 3]));
      ctl[8 + cw] = (unsigned)(4 * sub + 4);
#pragma unroll
      for (int t = WQ_FIRST + sub * WQ_STEP; t < WQ_FIRST + (sub + 1) * WQ_STEP && t < 16; ++t) W[t] = wqp[(size_t)t * (NH * DH / 8)];
      __builtin_amdgcn_sched_barrier(0);
    }
    STAMP(7);
    __builtin_amdgcn_sched_barrier(0);
    float qa[8];
#pragma unroll
    for (int j = 0; j < 8; ++j) qa[j] = 0.f;
#pragma unroll
    for (int t = 0; t < 16; ++t) {
      const unsigned xw = (t < 8) ? xa[t >> 1] : xb[(t - 8) >> 1];
      const float xv = (t & 1) ? bfhi(xw) : bflo(xw);
#pragma unroll
      for (int j = 0; j < 4; ++j) { qa[2 * j] = fmaf(xv, bflo(W[t][j]), qa[2 * j]); qa[2 * j + 1] = fmaf(xv, bfhi(W[t][j]), qa[2 * j + 1]); }
    }
#pragma unroll
    for (int j = 0; j < 8; ++j) qa[j] = xrow_sum(qa[j]);
    if (r == 0) {
      LAS f32x4* qp = (LAS f32x4*)(L + L_QPART) + cw * 32 + c * 2;
      qp[0] = f32x4{qa[0], qa[1], qa[2], qa[3]}; qp[1] = f32x4{qa[4], qa[5], qa[6], qa[7]};
    }
    wait_lgkm();
    STAMP(11);
    if (lane == 0) __hip_atomic_fetch_add((LAS unsigned*)(L + L_CTL) + 1, 1u, __ATOMIC_RELAXED, __HIP_MEMORY_SCOPE_WORKGROUP);
    for (unsigned spins = 0; spins < SPIN_CAP; ++spins) { if (__builtin_amdgcn_readfirstlane(ctl[1]) >= (unsigned)NCONS) break; }
    asm volatile("" ::: "memory");
    v4u q;
    {
      f32x4 s0 = {0.f, 0.f, 0.f, 0.f}, s1 = {0.f, 0.f, 0.f, 0.f};
#pragma unroll
      for (int ww = 0; ww < 8; ++ww) { const LAS f32x4* qp = (const LAS f32x4*)(L + L_QPART) + ww * 32 + c * 2; s0 += qp[0]; s1 += qp[1]; }
      q = v4u{pack_bf16(s0[0], s0[1]), pack_bf16(s0[2], s0[3]), pack_bf16(s1[0], s1[1]), pack_bf16(s1[2], s1[3])};
    }
    if (cw == 0) { STAMP(1); STAMP_OUT(1); STAMP_OUT(6); STAMP_OUT(7); STAMP_OUT(11); }
    LAS float* sc = (LAS float*)(L + L_SC) + cw * 512 + lane;
    float mx;
    {
      float x[16];
#pragma unroll
      for (int t = 0; t < 16; ++t) x[t] = dot8(P[t], q);
      const float s = treduce16(x, c); sc[0] = s; mx = s;
    }
    const GAS v4u* wop = (const GAS v4u*)(wo + (size_t)(h * DH + 32 * r + (c & 1)) * DM + 64 * cw + 8 * (c >> 1));
    auto kgroup = [&](int g) {
      float x[16];
#pragma unroll
      for (int sub = 0; sub < 4; ++sub) {
        wait_landed(ctl, (unsigned)(8 * (16 * g + 4 * sub + 3) + cw + 1), have);
        v4u d[4];
#pragma unroll
        for (int i = 0; i < 4; ++i) d[i] = rp[(4 * sub + i) * 512];
        wait_lgkm();
        asm volatile("" : "+v"(d[0]), "+v"(d[1]), "+v"(d[2]), "+v"(d[3]));
        ctl[8 + cw] = (unsigned)(16 * g + 4 * sub + 4);
#pragma unroll
        for (int i = 0; i < 4; ++i) x[4 * sub + i] = dot8(d[i], q);
      }
      const float s = treduce16(x, c); sc[g * 64] = s; mx = fmaxf(mx, s);
    };
    v4u Wo[16];
#pragma unroll 1
    for (int g = 1; g < 3; ++g) kgroup(g);
#pragma unroll
    for (int t = 0; t < 8; ++t) Wo[t] = wop[t * (2 * DM / 8)];
    __builtin_amdgcn_sched_barrier(0);
#pragma unroll 1
    for (int g = 3; g < 5; ++g) kgroup(g);
#pragma unroll
    for (int t = 8; t < 16; ++t) Wo[t] = wop[t * (2 * DM / 8)];
    __builtin_amdgcn_sched_barrier(0);
#pragma unroll 1
    for (int g = 5; g < 8; ++g) kgroup(g);
    LAS float* ml = (LAS float*)(L + L_ML);
    {
      const float m = wave_max(mx);
      if (lane == 0) ml[cw] = m;
      wait_lgkm();
      if (lane == 0) __hip_atomic_fetch_add((LAS unsigned*)(L + L_CTL) + 3, 1u, __ATOMIC_RELAXED, __HIP_MEMORY_SCOPE_WORKGROUP);
    }
    unsigned Wp[16][4];
    {
      const bool odd = (c & 1) != 0;
      const unsigned sel_lo = odd ? 0x01000504u : 0x05040100u, sel_hi = odd ? 0x03020706u : 0x07060302u;
#pragma unroll
      for (int i = 0; i < 16; ++i)
#pragma unroll
        for (int j = 0; j < 2; ++j) {
          const unsigned keep = odd ? Wo[i][2 + j] : Wo[i][j], send = odd ? Wo[i][j] : Wo[i][2 + j];
          const unsigned recv = (unsigned)__builtin_amdgcn_mov_dpp((int)send, XOR1, 0xf, 0xf, true);
          Wp[i][2 * j] = __builtin_amdgcn_perm(recv, keep, sel_lo);
          Wp[i][2 * j + 1] = __builtin_amdgcn_perm(recv, keep, sel_hi);
          asm volatile("" : "+v"(Wp[i][2 * j]), "+v"(Wp[i][2 * j + 1]));
        }
    }
    for (unsigned spins = 0; spins < SPIN_CAP; ++spins) { if (__builtin_amdgcn_readfirstlane(ctl[3]) >= (unsigned)NCONS) break; }
    asm volatile("" ::: "memory");
    {
      const f32x4 m0 = *(const LAS f32x4*)ml, m1 = *(const LAS f32x4*)(ml + 4);
      const float M = fmaxf(fmaxf(fmaxf(m0[0], m0[1]), fmaxf(m0[2], m0[3])), fmaxf(fmaxf(m1[0], m1[1]), fmaxf(m1[2], m1[3])));
      const float mc = M * SM_C;
      float lsum = 0.f;
#pragma unroll
      for (int g = 0; g < 8; ++g) { const float p = __builtin_amdgcn_exp2f(fmaf(sc[g * 64], SM_C, -mc)); sc[g * 64] = p; lsum += p; }
      lsum = wave_sum(lsum);
      if (lane == 0) ml[8 + cw] = lsum;
    }
    f32x2 acc2[4];
#pragma unroll
    for (int j = 0; j < 4; ++j) acc2[j] = f32x2{0.f, 0.f};
    auto vgroup = [&](int g) {
      const float p = sc[g * 64];
      Unroll<0, 4>::run([&](auto S) {
        constexpr int sub = decltype(S)::value;
        wait_landed(ctl, (unsigned)(1024 + 8 * (16 * g + 4 * sub + 3) + cw + 1), have);
        v4u d[4];
#pragma unroll
        for (int i = 0; i < 4; ++i) d[i] = rp[(4 * sub + i) * 512];
        wait_lgkm();
        asm volatile("" : "+v"(d[0]), "+v"(d[1]), "+v"(d[2]), "+v"(d[3]));
        ctl[8 + cw] = (unsigned)(128 + 16 * g + 4 * sub + 4);
        pv_step<4 * sub + 0>(acc2, d[0], p); pv_step<4 * sub + 1>(acc2, d[1], p); pv_step<4 * sub + 2>(acc2, d[2], p); pv_step<4 * sub + 3>(acc2, d[3], p);
        asm volatile("" : "+v"(acc2[0]), "+v"(acc2[1]), "+v"(acc2[2]), "+v"(acc2[3]));
      });
    };
#pragma unroll 1
    for (int g = 0; g < 7; ++g) vgroup(g);
    for (unsigned spins = 0; spins < SPIN_CAP; ++spins) { if (__builtin_amdgcn_readfirstlane(ctl[4]) != 0u) break; __builtin_amdgcn_s_sleep(1); }
    asm volatile("" ::: "memory");
    const int col = 64 * cw + 4 * c + r;
    float ymoe = ((const LAS float*)(L + L_YMOE))[col];
    float gate = ((const LAS float*)(L + L_SCAL))[0];
    unsigned tag32 = f2u(((const LAS float*)(L + L_SCAL))[2]);
    GAS u64* dst = (GAS u64*)(ws + WS_INBOX) + ((size_t)(b * NH + (col >> 4)) * NH + h) * 16 + (col & 15);
    asm volatile("" : "+v"(ymoe), "+v"(gate), "+v"(tag32), "+v"(dst));
    vgroup(7);
    {
      float u[4];
#pragma unroll
      for (int j = 0; j < 2; ++j)
#pragma unroll
        for (int e = 0; e < 2; ++e) {
          auto t = __builtin_amdgcn_permlane32_swap(f2u(acc2[j][e]), f2u(acc2[j + 2][e]), false, false);
          u[2 * j + e] = u2f(t[0]) + u2f(t[1]);
        }
      f32x2 o2;
#pragma unroll
      for (int e = 0; e < 2; ++e) {
        auto t = __builtin_amdgcn_permlane16_swap(f2u(u[e]), f2u(u[2 + e]), false, false);
        o2[e] = u2f(t[0]) + u2f(t[1]);
      }
      *(LAS f32x2*)(L + L_QPART + (cw * 128 + 8 * c + 2 * r) * 4) = o2;
    }
    raw_barrier();
    if (cw == 0) { STAMP(4); STAMP_OUT(4); }
    {
      f32x2 o2 = {0.f, 0.f};
#pragma unroll
      for (int ww = 0; ww < 8; ++ww) o2 += *(const LAS f32x2*)(L + L_QPART + (ww * 128 + 2 * lane) * 4);
      const f32x4 l0 = *(const LAS f32x4*)(ml + 8), l1 = *(const LAS f32x4*)(ml + 12);
      const float inv = 1.0f / (((l0[0] + l0[1]) + (l0[2] + l0[3])) + ((l1[0] + l1[1]) + (l1[2] + l1[3])));
      const float op = u2f(pack_bf16(o2[0] * inv, o2[1] * inv));
      float y[4] = {0.f, 0.f, 0.f, 0.f};
      Unroll<0, 16>::run([&](auto I) {
        constexpr int i = decltype(I)::value;
        const unsigned os = f2u(dpp<0x150 + i>(op));
#pragma unroll
        for (int j = 0; j < 4; ++j) y[j] = dot2(os, Wp[i][j], y[j]);
      });
      auto t0 = __builtin_amdgcn_permlane32_swap(f2u(y[0]), f2u(y[2]), false, false);
      auto t1 = __builtin_amdgcn_permlane32_swap(f2u(y[1]), f2u(y[3]), false, false);
      const float u0 = u2f(t0[0]) + u2f(t0[1]), u1 = u2f(t1[0]) + u2f(t1[1]);
      auto t2 = __builtin_amdgcn_permlane16_swap(f2u(u0), f2u(u1), false, false);
      const float ycol = u2f(t2[0]) + u2f(t2[1]);
      __hip_atomic_store(dst, ((u64)tag32 << 32) | f2u(fmaf(ycol, gate, ymoe)), RLX_AGENT);
      if (cw == 0) { STAMP(5); STAMP_OUT(5); }
    }
    return;
  } else if (w == NCONS + 2) {
    asm volatile("s_barrier" ::: "memory");
    const unsigned tagb = ((unsigned)__hip_atomic_load((GAS u64*)(ws + WS_EPOCH), RLX_AGENT) + 1u) & 0xffffu;
    GAS unsigned* board = (GAS unsigned*)(ws + WS_PROG);
    for (unsigned it = 0; it < 4096; ++it) {
      __builtin_amdgcn_s_sleep(GOV_SLEEP);
      const unsigned my = __builtin_amdgcn_readfirstlane(ctl[0]);
      if (my >= (unsigned)(NPIECE - 192)) break;
      const unsigned me = (my - (unsigned)((__builtin_amdgcn_s_memrealtime() * 287ull) >> 10)) & 0xffffu;
      if (lane == 0) __hip_atomic_store(board + bid * 32, (tagb << 16) | me, RLX_AGENT);
      float sum = 0.f, cnt = 0.f;
      if (lane < NPEER) {
        const unsigned ej = __hip_atomic_load(board + ((bid + 1 + 33 * lane + 7 * it) & 255) * 32, RLX_AGENT);
        const bool ok = (ej >> 16) == tagb; sum = ok ? (float)(short)(u16)(me - ej) : 0.f; cnt = ok ? 1.f : 0.f;
      }
      sum = row_sum(sum); cnt = row_sum(cnt);
      const float diff = u2f(__builtin_amdgcn_readfirstlane(f2u(sum / fmaxf(cnt, 1.f))));
      cnt = u2f(__builtin_amdgcn_readfirstlane(f2u(cnt)));
      const unsigned cls = (cnt < (float)(NPEER / 2)) ? 0u : (diff >= (float)TH2) ? 1u : (diff >= (float)TH1) ? 2u : (diff <= (float)-TH2) ? 4u : (diff <= (float)-TH1) ? 3u : 0u;
      if (lane == 0) ctl[2] = cls;
    }
    raw_barrier();
  } else {
    asm volatile("s_barrier" ::: "memory");
    GAS u64* epoch = (GAS u64*)(ws + WS_EPOCH) + b * 16;
    const u64 e0 = __hip_atomic_load(epoch, RLX_AGENT);
    const GAS v4u* xp = (const GAS v4u*)(xg + b * DM + 8 * lane);
    const v4u xv = xp[0];
    const v4u gw = *(const GAS v4u*)(gate_w + 8 * lane);
    const v4u wr = *(const GAS v4u*)(w_r + 8 * lane);
    const u64 tag64 = e0 + 1ull;
    const unsigned tag32 = (unsigned)tag64 ^ (unsigned)(tag64 >> 32);
    {
      const __amdgpu_buffer_rsrc_t rsI = __builtin_amdgcn_make_buffer_rsrc((void*)(ws + WS_INBOX + (size_t)(b * NH + h) * 4096), 0, 4096, 0x00020000);
      const v4u inv = {0u, ~tag32, 0u, ~tag32};
#pragma unroll
      for (int i = 0; i < 4; ++i) __builtin_amdgcn_raw_buffer_store_b128(inv, rsI, lane * 16 + i * 1024, 0, 16);
      wait_vm<0>();
      if (lane == 0) __hip_atomic_store((GAS u64*)(ws + WS_READY) + b * NH + h, tag64, RLX_AGENT);
    }
    LAS float* xs = (LAS float*)(L + L_XS);
    { LAS f32x4* xs4 = (LAS f32x4*)xs + 2 * lane;
      xs4[0] = f32x4{bflo(xv.x), bfhi(xv.x), bflo(xv.y), bfhi(xv.y)}; xs4[1] = f32x4{bflo(xv.z), bfhi(xv.z), bflo(xv.w), bfhi(xv.w)}; }
    float sg = 0.f, sr = 0.f;
#pragma unroll
    for (int j = 0; j < 4; ++j) {
      sg += rbf(bflo(xv[j]) * bflo(gw[j])); sg += rbf(bfhi(xv[j]) * bfhi(gw[j]));
      sr = fmaf(bflo(xv[j]), bflo(wr[j]), sr); sr = fmaf(bfhi(xv[j]), bfhi(wr[j]), sr);
    }
    sg = rbf(wave_sum(sg)); sr = rbf(wave_sum(sr));
    const float g = rbf(sigmoidf_(sg)), rs = rbf(sigmoidf_(sr)), omg = rbf(1.0f - g);
    if (lane == 0) { LAS float* sca = (LAS float*)(L + L_SCAL); sca[0] = g; sca[1] = omg; sca[2] = u2f(tag32); ((LAS u64*)sca)[2] = tag64; }
    float y[8];
#pragma unroll
    for (int j = 0; j < 8; ++j) y[j] = 0.f;
    if (h < FF / FS) {
      for (unsigned spins = 0; spins < SPIN_CAP; ++spins) { if (__builtin_amdgcn_readfirstlane(ctl[1]) >= (unsigned)NCONS) break; __builtin_amdgcn_s_sleep(8); }
      const int rr = lane >> 3, cc = lane & 7;
      const GAS v4u* gp = (const GAS v4u*)(w_gate + (size_t)(64 * rr) * FF + h * FS + 8 * cc);
      const GAS v4u* up = (const GAS v4u*)(w_up + (size_t)(64 * rr) * FF + h * FS + 8 * cc);
      float ag[8], au[8];
#pragma unroll
      for (int j = 0; j < 8; ++j) { ag[j] = 0.f; au[j] = 0.f; }
#pragma unroll 1
      for (int it = 0; it < 16; ++it) {
        v4u Gc[4], Uc[4];
#pragma unroll
        for (int t = 0; t < 4; ++t) { Gc[t] = gp[(it * 4 + t) * (FF / 8)]; Uc[t] = up[(it * 4 + t) * (FF / 8)]; }
        const f32x4 xq = *(const LAS f32x4*)(xs + 64 * rr + 4 * it);
#pragma unroll
        for (int t = 0; t < 4; ++t) {
          const float xt = xq[t];
#pragma unroll
          for (int j = 0; j < 4; ++j) {
            ag[2 * j] = fmaf(xt, bflo(Gc[t][j]), ag[2 * j]); ag[2 * j + 1] = fmaf(xt, bfhi(Gc[t][j]), ag[2 * j + 1]);
            au[2 * j] = fmaf(xt, bflo(Uc[t][j]), au[2 * j]); au[2 * j + 1] = fmaf(xt, bfhi(Uc[t][j]), au[2 * j + 1]);
          }
        }
        __builtin_amdgcn_s_sleep(MOE_SLEEP);
      }
      LAS float* act = (LAS float*)(L + L_ACT);
#pragma unroll
      for (int j = 0; j < 8; ++j) {
        float gg = ag[j], uu = au[j];
        gg += dpp<XOR8>(gg); uu += dpp<XOR8>(uu);
        gg = rbf(xrow_sum(gg)); uu = rbf(xrow_sum(uu));
        const float si = rbf(gg * sigmoidf_(gg));
        if (lane < 8) act[8 * lane + j] = rbf(si * uu);
      }
      const GAS v4u* dp = (const GAS v4u*)(w_down + (size_t)(h * FS) * DM + lane * 8);
#pragma unroll 1
      for (int it = 0; it < 8; ++it) {
        v4u Dc[8];
#pragma unroll
        for (int t = 0; t < 8; ++t) Dc[t] = dp[(it * 8 + t) * (DM / 8)];
#pragma unroll
        for (int t = 0; t < 8; ++t) {
          const float a = act[it * 8 + t];
#pragma unroll
          for (int j = 0; j < 4; ++j) { y[2 * j] = fmaf(a, bflo(Dc[t][j]), y[2 * j]); y[2 * j + 1] = fmaf(a, bfhi(Dc[t][j]), y[2 * j + 1]); }
        }
        __builtin_amdgcn_s_sleep(MOE_SLEEP);
      }
    }
    const float f = omg * rs;
    LAS f32x4* ym = (LAS f32x4*)(L + L_YMOE) + 2 * lane;
    ym[0] = f32x4{y[0] * f, y[1] * f, y[2] * f, y[3] * f}; ym[1] = f32x4{y[4] * f, y[5] * f, y[6] * f, y[7] * f};
    {
      GAS u64* rdy = (GAS u64*)(ws + WS_READY) + b * NH + (lane & 31);
      for (unsigned spins = 0; spins < SPIN_CAP; ++spins) {
        const u64 v = __hip_atomic_load(rdy, RLX_AGENT);
        if (__all(v == tag64)) break;
        __builtin_amdgcn_s_sleep(8);
      }
    }
    wait_lgkm();
    if (lane == 0) ctl[4] = 1u;
    STAMP(9); STAMP_OUT(9);
    raw_barrier();
  }
  {
    const unsigned tag32 = f2u(((const LAS float*)(L + L_SCAL))[2]);
    const float omg = ((const LAS float*)(L + L_SCAL))[1];
    GAS u64* src = (GAS u64*)(ws + WS_INBOX) + ((size_t)(b * NH + h) * NH + 8 * r) * 16 + c;
    const float xres = bflo((unsigned)xg[b * DM + 16 * h + c]);
    if (w == NCONS + 1) __builtin_amdgcn_s_sleep(POLL_STAG); else if (w == NCONS + 2) __builtin_amdgcn_s_sleep(2 * POLL_STAG);
    unsigned v[8];
    for (unsigned spins = 0; spins < SPIN_CAP; ++spins) {
      bool ok = true;
#pragma unroll
      for (int i = 0; i < 8; ++i) { const u64 x = __hip_atomic_load(src + i * 16, RLX_AGENT); v[i] = (unsigned)x; ok &= (unsigned)(x >> 32) == tag32; }
      if (__all(ok)) break;
    }
    float s = 0.f;
#pragma unroll
    for (int i = 0; i < 8; ++i) s += u2f(v[i]);
    s = xrow_sum(s);
    s = fmaf(omg, xres, s);
    const unsigned o2 = pack_bf16(s, dpp<XOR1>(s));
    if (lane < 16 && (lane & 1) == 0) *(GAS unsigned*)(out + b * DM + 16 * h + lane) = o2;
    if (w == 0 && h == 0 && lane == 0) __hip_atomic_store((GAS u64*)(ws + WS_EPOCH) + b * 16, ((const LAS u64*)(L + L_SCAL))[2], RLX_AGENT);
  }
}
}

extern "C" void kernel_launch(void* const* d_in, const int* in_sizes, int n_in, void* d_out, int out_size, void* d_ws, size_t ws_size, hipStream_t stream) {
  (void)in_sizes; (void)n_in; (void)out_size; (void)ws_size;
  (void)hipFuncSetAttribute((const void*)fused_kernel, hipFuncAttributeMaxDynamicSharedMemorySize, LDS_BYTES);
  fused_kernel<<<dim3(NB * NH), dim3(NTHR), LDS_BYTES, stream>>>(
      (const u16*)d_in[0], (const u16*)d_in[1], (const u16*)d_in[2], (const u16*)d_in[3], (const u16*)d_in[4],
      (const u16*)d_in[6], (const u16*)d_in[7], (const u16*)d_in[8], (const u16*)d_in[9], (const u16*)d_in[10], (u16*)d_out, (unsigned char*)d_ws);
}
```

```cpp
#define LEAK 0
#define LEAK_OFF 0
#define LEAK_BID 9
#include <hip/hip_runtime.h>
#include <stdint.h>
#include <type_traits>

#define GAS __attribute__((address_space(1)))
#define LAS __attribute__((address_space(3)))
typedef unsigned short u16;
typedef unsigned long long u64;
typedef unsigned v4u __attribute__((ext_vector_type(4)));
typedef unsigned v2u __attribute__((ext_vector_type(2)));
typedef float f32x4 __attribute__((ext_vector_type(4)));
typedef float f32x2 __attribute__((ext_vector_type(2)));
typedef __bf16 bf16x2 __attribute__((ext_vector_type(2)));

#define STAMP(i)
#define STAMP_OUT(i)
#define INFL 24
#define W_M2 22
#define W_M1 24
#define W_P1 32
#define W_P2 44
#define TH1 10
#define TH2 28
#define BLK 32
#define POLL_STAG 4
#define NPEER 4
#define GOV_SLEEP 100
#define WQ_FIRST 4
#define WQ_STEP 4
#define MOE_SLEEP 32
#define BOOT 32
#define BOOT_END 256

namespace {
constexpr int NB = 8, NH = 32, SK = 4096, DH = 128, DM = 512, FF = 1792, FS = 64;
constexpr int NCONS = 8, NWAVES = NCONS + 3, NTHR = 64 * NWAVES;
constexpr float SM_C = 0.088388347648318440f * 1.4426950408889634f;
constexpr int NPIECE = 2048;
constexpr int RING = 128;
constexpr unsigned SPIN_CAP = 1u << 22;

constexpr int L_RING = 0;
constexpr int L_SC = RING * 1024;
constexpr int L_QPART = L_SC + 8 * 512 * 4;
constexpr int L_ML = L_QPART + 8 * 128 * 4;
constexpr int L_CTL = L_ML + 64;
constexpr int L_SCAL = L_CTL + 64;
constexpr int L_ACT = L_SCAL + 64;
constexpr int L_YMOE = L_ACT + 256;
constexpr int L_XS = L_YMOE + 512 * 4;
constexpr int LDS_BYTES = L_XS + 512 * 4;
static_assert(LDS_BYTES <= 160 * 1024, "lds");

constexpr size_t WS_EPOCH = 0;
constexpr size_t WS_READY = 4096;
constexpr size_t WS_PROG = 32768;
constexpr size_t WS_INBOX = 65536;

#define RLX_AGENT __ATOMIC_RELAXED, __HIP_MEMORY_SCOPE_AGENT

__device__ __forceinline__ float u2f(unsigned u) { return __builtin_bit_cast(float, u); }
__device__ __forceinline__ unsigned f2u(float f) { return __builtin_bit_cast(unsigned, f); }
__device__ __forceinline__ float bflo(unsigned u) { return u2f(u << 16); }
__device__ __forceinline__ float bfhi(unsigned u) { return u2f(u & 0xffff0000u); }
__device__ __forceinline__ float dot2(unsigned a, unsigned b, float acc) {
  return __builtin_amdgcn_fdot2_f32_bf16(__builtin_bit_cast(bf16x2, a), __builtin_bit_cast(bf16x2, b), acc, false);
}
__device__ __forceinline__ unsigned pack_bf16(float lo, float hi) {
  bf16x2 v = {(__bf16)lo, (__bf16)hi};
  return __builtin_bit_cast(unsigned, v);
}
__device__ __forceinline__ float rbf(float x) { return u2f((unsigned)__builtin_bit_cast(u16, (__bf16)x) << 16); }
template <int CTRL> __device__ __forceinline__ float dpp(float x) {
  return __builtin_bit_cast(float, __builtin_amdgcn_mov_dpp(__builtin_bit_cast(int, x), CTRL, 0xf, 0xf, true));
}
constexpr int XOR1 = 0xB1, XOR2 = 0x4E, XOR7 = 0x141, XOR8 = 0x128;
__device__ __forceinline__ float xrow_sum(float x) {
  auto s = __builtin_amdgcn_permlane16_swap(f2u(x), f2u(x), false, false);
  x = u2f(s[0]) + u2f(s[1]);
  auto t = __builtin_amdgcn_permlane32_swap(f2u(x), f2u(x), false, false);
  return u2f(t[0]) + u2f(t[1]);
}
__device__ __forceinline__ float xrow_max(float x) {
  auto s = __builtin_amdgcn_permlane16_swap(f2u(x), f2u(x), false, false);
  x = fmaxf(u2f(s[0]), u2f(s[1]));
  auto t = __builtin_amdgcn_permlane32_swap(f2u(x), f2u(x), false, false);
  return fmaxf(u2f(t[0]), u2f(t[1]));
}
__device__ __forceinline__ float row_sum(float x) { x += dpp<XOR8>(x); x += dpp<XOR7>(x); x += dpp<XOR2>(x); x += dpp<XOR1>(x); return x; }
__device__ __forceinline__ float row_max(float x) { x = fmaxf(x, dpp<XOR8>(x)); x = fmaxf(x, dpp<XOR7>(x)); x = fmaxf(x, dpp<XOR2>(x)); x = fmaxf(x, dpp<XOR1>(x)); return x; }
__device__ __forceinline__ float wave_sum(float x) { return xrow_sum(row_sum(x)); }
__device__ __forceinline__ float wave_max(float x) { return xrow_max(row_max(x)); }
__device__ __forceinline__ float dot8(const v4u k, const v4u q) {
  float d = dot2(k.x, q.x, 0.f); d = dot2(k.y, q.y, d); d = dot2(k.z, q.z, d); d = dot2(k.w, q.w, d); return d;
}
__device__ __forceinline__ float treduce16(float (&x)[16], int c) {
#define TR_STEP(HALF, CTRL, BIT) _Pragma("unroll") for (int h = 0; h < (HALF); ++h) { \
    const float lo_ = x[h], hi_ = x[h + (HALF)]; const float t1_ = lo_ + dpp<CTRL>(lo_), t2_ = hi_ + dpp<CTRL>(hi_); x[h] = (c & (BIT)) ? t2_ : t1_; }
  TR_STEP(8, XOR8, 8) TR_STEP(4, XOR7, 4) TR_STEP(2, XOR2, 2) TR_STEP(1, XOR1, 1)
#undef TR_STEP
  return x[0];
}
__device__ __forceinline__ float sigmoidf_(float v) { return 1.0f / (1.0f + __expf(-v)); }
template <int T> __device__ __forceinline__ void pv_step(f32x2 (&acc)[4], const v4u v, float p) {
  const float pb = dpp<0x150 + T>(p);
  const f32x2 p2 = {pb, pb};
#pragma unroll
  for (int j = 0; j < 4; ++j) acc[j] = __builtin_elementwise_fma(p2, f32x2{bflo(v[j]), bfhi(v[j])}, acc[j]);
}
template <int T, int N> struct Unroll { template <class F> static __device__ __forceinline__ void run(F&& f) { f(std::integral_constant<int, T>{}); Unroll<T + 1, N>::run(f); } };
template <int N> struct Unroll<N, N> { template <class F> static __device__ __forceinline__ void run(F&&) {} };
template <int N> __device__ __forceinline__ void wait_vm() { asm volatile("s_waitcnt vmcnt(%0)" ::"n"(N) : "memory"); }
__device__ __forceinline__ void wait_lgkm() { asm volatile("s_waitcnt lgkmcnt(0)" ::: "memory"); }
__device__ __forceinline__ void raw_barrier() { asm volatile("s_waitcnt lgkmcnt(0)\n\ts_barrier" ::: "memory"); }

__device__ __forceinline__ void wait_landed(volatile LAS unsigned* ctl, unsigned need, unsigned& have) {
  if (have >= need) return;
  for (unsigned spins = 0; spins < SPIN_CAP; ++spins) {
    have = __builtin_amdgcn_readfirstlane(ctl[0]);
    if (have >= need) break;
    __builtin_amdgcn_s_sleep(1);
  }
  asm volatile("" ::: "memory");
}

__device__ __forceinline__ int min_consumed(volatile LAS unsigned* ctl, int lane) {
  unsigned cn = ctl[8 + (lane & 7)];
  cn = min(cn, (unsigned)__builtin_amdgcn_mov_dpp((int)cn, XOR1, 0xf, 0xf, true));
  cn = min(cn, (unsigned)__builtin_amdgcn_mov_dpp((int)cn, XOR2, 0xf, 0xf, true));
  cn = min(cn, (unsigned)__builtin_amdgcn_mov_dpp((int)cn, XOR7, 0xf, 0xf, true));
  return (int)__builtin_amdgcn_readfirstlane(cn);
}

__global__ void __launch_bounds__(NTHR) fused_kernel(const u16* __restrict__ xg, const u16* __restrict__ Kg, const u16* __restrict__ Vg, const u16* __restrict__ wq,
                                                     const u16* __restrict__ wo, const u16* __restrict__ w_r, const u16* __restrict__ w_gate, const u16* __restrict__ w_up,
                                                     const u16* __restrict__ w_down, const u16* __restrict__ gate_w, u16* __restrict__ out, unsigned char* __restrict__ ws) {
  extern __shared__ __attribute__((aligned(16))) unsigned char smem[];
  LAS unsigned char* L = (LAS unsigned char*)smem;
  const int tid = threadIdx.x, lane = tid & 63, w = __builtin_amdgcn_readfirstlane(tid >> 6);
  const int bid = blockIdx.x;
  const int h = (bid & 7) * 4 + (bid >> 6), b = (bid >> 3) & 7;
  const int r = lane >> 4, c = lane & 15;
  volatile LAS unsigned* ctl = (volatile LAS unsigned*)(L + L_CTL);
  STAMP(0);

  const u64 lk_t0 = __builtin_amdgcn_s_memrealtime(); u64 lk_x = lk_t0;
  if (__builtin_expect(w == 0, 1)) {
    __builtin_amdgcn_s_setprio(3);
    const size_t kvoff = (size_t)(b * NH + h) * SK * DH;
    const __amdgpu_buffer_rsrc_t rsK = __builtin_amdgcn_make_buffer_rsrc((void*)(Kg + kvoff), 0, SK * DH * 2, 0x00020000);
    const __amdgpu_buffer_rsrc_t rsV = __builtin_amdgcn_make_buffer_rsrc((void*)(Vg + kvoff), 0, SK * DH * 2, 0x00020000);
    const int voff = lane * 16;
    if (lane < 16) ctl[lane] = 0u;
#pragma unroll
    for (int i = 0; i < INFL; ++i) __builtin_amdgcn_raw_ptr_buffer_load_lds(rsK, (LAS void*)(L + L_RING + i * 1024), 16, voff, i * 1024, 0, 2);
    STAMP(10);
    raw_barrier();
    STAMP(11);
    int free_until = RING;
    auto make_room = [&](int gp) {
      if (gp + 4 > free_until) {
        free_until = 8 * min_consumed(ctl, lane) + RING;
        if (gp + 4 > free_until) {
          wait_vm<0>();
          ctl[0] = (unsigned)gp;
          for (unsigned spins = 0; spins < SPIN_CAP; ++spins) {
            free_until = 8 * min_consumed(ctl, lane) + RING;
            if (gp + 4 <= free_until) break;
            __builtin_amdgcn_s_sleep(1);
          }
        }
        asm volatile("" ::: "memory");
      }
    };
    for (int p = INFL; p < BOOT_END; p += 4) {
      make_room(p);
      LAS unsigned char* dst = L + L_RING + (p & (RING - 1)) * 1024;
#pragma unroll
      for (int i = 0; i < 4; ++i) __builtin_amdgcn_raw_ptr_buffer_load_lds(rsK, (LAS void*)(dst + i * 1024), 16, voff, (p + i) * 1024, 0, 2);
      wait_vm<BOOT>();
      if (LEAK == 4 && p + 4 > BOOT && lk_x == lk_t0) lk_x = __builtin_amdgcn_s_memrealtime();
      if (LEAK == 3 && p == 128 + BOOT - 4) lk_x = __builtin_amdgcn_s_memrealtime();
      if (p + 4 > BOOT) ctl[0] = (unsigned)(p + 4 - BOOT);
    }
    int pub = BOOT_END - BOOT;
    auto run_block = [&](auto WW, const __amdgpu_buffer_rsrc_t rs, int goff, int p0, int p1) {
      constexpr int W = decltype(WW)::value;
#pragma unroll 1
      for (int p = p0; p < p1; p += 4) {
        const int gp = goff + p;
        make_room(gp);
        LAS unsigned char* dst = L + L_RING + (gp & (RING - 1)) * 1024;
#pragma unroll
        for (int i = 0; i < 4; ++i) __builtin_amdgcn_raw_ptr_buffer_load_lds(rs, (LAS void*)(dst + i * 1024), 16, voff, (p + i) * 1024, 0, 2);
        wait_vm<W>();
        pub = max(pub, gp + 4 - W);
        ctl[0] = (unsigned)pub;
      }
    };
    for (int half = 0; half < 2; ++half) {
      const __amdgpu_buffer_rsrc_t rs = half ? rsV : rsK;
#pragma unroll 1
      for (int p = half ? 0 : BOOT_END; p < 1024; p += BLK) {
        const int cl = __builtin_amdgcn_readfirstlane(ctl[2]);
        if (cl == 0) run_block(std::integral_constant<int, INFL>{}, rs, half * 1024, p, p + BLK);
        else if (cl == 1) run_block(std::integral_constant<int, W_M2>{}, rs, half * 1024, p, p + BLK);
        else if (cl == 2) run_block(std::integral_constant<int, W_M1>{}, rs, half * 1024, p, p + BLK);
        else if (cl == 3) run_block(std::integral_constant<int, W_P1>{}, rs, half * 1024, p, p + BLK);
        else run_block(std::integral_constant<int, W_P2>{}, rs, half * 1024, p, p + BLK);
      }
      if (half == 0) { STAMP(2); if (LEAK == 2) lk_x = __builtin_amdgcn_s_memrealtime(); }
    }
    wait_vm<0>();
    ctl[0] = (unsigned)NPIECE;
    if (LEAK == 5) lk_x = __builtin_amdgcn_s_memrealtime();
    STAMP(3);
    STAMP_OUT(0); STAMP_OUT(2); STAMP_OUT(3); STAMP_OUT(10); STAMP_OUT(11); STAMP_OUT(12);
    raw_barrier();
  } else if (w <= NCONS) {
    const int cw = w - 1;
    asm volatile("s_barrier" ::: "memory");
    const GAS v4u* xp = (const GAS v4u*)(xg + b * DM + 64 * cw + 16 * r);
    const v4u xa = xp[0], xb = xp[1];
    const GAS v4u* wqp = (const GAS v4u*)(wq + (size_t)(64 * cw + 16 * r) * (NH * DH) + h * DH + 8 * c);
    v4u W[16];
    STAMP(6);
    const LAS v4u* rp = (const LAS v4u*)(L + L_RING + cw * 1024) + lane;
    unsigned have = 0;
    v4u P[16];
#pragma unroll
    for (int t = 0; t < WQ_FIRST; ++t) W[t] = wqp[(size_t)t * (NH * DH / 8)];
    __builtin_amdgcn_sched_barrier(0);
#pragma unroll
    for (int sub = 0; sub < 4; ++sub) {
      wait_landed(ctl, (unsigned)(8 * (4 * sub + 3) + cw + 1), have);
#pragma unroll
      for (int i = 0; i < 4; ++i) P[4 * sub + i] = rp[(4 * sub + i) * 512];
      wait_lgkm();
      asm volatile("" : "+v"(P[4 * sub]), "+v"(P[4 * sub + 1]), "+v"(P[4 * sub + 2]), "+v"(P[4 * sub + 3]));
      ctl[8 + cw] = (unsigned)(4 * sub + 4);
#pragma unroll
      for (int t = WQ_FIRST + sub * WQ_STEP; t < WQ_FIRST + (sub + 1) * WQ_STEP && t < 16; ++t) W[t] = wqp[(size_t)t * (NH * DH / 8)];
      __builtin_amdgcn_sched_barrier(0);
    }
    STAMP(7);
    __builtin_amdgcn_sched_barrier(0);
    float qa[8];
#pragma unroll
    for (int j = 0; j < 8; ++j) qa[j] = 0.f;
#pragma unroll
    for (int t = 0; t < 16; ++t) {
      const unsigned xw = (t < 8) ? xa[t >> 1] : xb[(t - 8) >> 1];
      const float xv = (t & 1) ? bfhi(xw) : bflo(xw);
#pragma unroll
      for (int j = 0; j < 4; ++j) { qa[2 * j] = fmaf(xv, bflo(W[t][j]), qa[2 * j]); qa[2 * j + 1] = fmaf(xv, bfhi(W[t][j]), qa[2 * j + 1]); }
    }
#pragma unroll
    for (int j = 0; j < 8; ++j) qa[j] = xrow_sum(qa[j]);
    if (r == 0) {
      LAS f32x4* qp = (LAS f32x4*)(L + L_QPART) + cw * 32 + c * 2;
      qp[0] = f32x4{qa[0], qa[1], qa[2], qa[3]}; qp[1] = f32x4{qa[4], qa[5], qa[6], qa[7]};
    }
    wait_lgkm();
    if (lane == 0) __hip_atomic_fetch_add((LAS unsigned*)(L + L_CTL) + 1, 1u, __ATOMIC_RELAXED, __HIP_MEMORY_SCOPE_WORKGROUP);
    for (unsigned spins = 0; spins < SPIN_CAP; ++spins) { if (__builtin_amdgcn_readfirstlane(ctl[1]) >= (unsigned)NCONS) break; }
    asm volatile("" ::: "memory");
    v4u q;
    {
      f32x4 s0 = {0.f, 0.f, 0.f, 0.f}, s1 = {0.f, 0.f, 0.f, 0.f};
#pragma unroll
      for (int ww = 0; ww < 8; ++ww) { const LAS f32x4* qp = (const LAS f32x4*)(L + L_QPART) + ww * 32 + c * 2; s0 += qp[0]; s1 += qp[1]; }
      q = v4u{pack_bf16(s0[0], s0[1]), pack_bf16(s0[2], s0[3]), pack_bf16(s1[0], s1[1]), pack_bf16(s1[2], s1[3])};
    }
    if (cw == 0) { STAMP(1); STAMP_OUT(1); STAMP_OUT(6); STAMP_OUT(7); }
    LAS float* sc = (LAS float*)(L + L_SC) + cw * 512 + lane;
    float mx;
    {
      float x[16];
#pragma unroll
      for (int t = 0; t < 16; ++t) x[t] = dot8(P[t], q);
      const float s = treduce16(x, c); sc[0] = s; mx = s;
    }
    const GAS v4u* wop = (const GAS v4u*)(wo + (size_t)(h * DH + 32 * r + (c & 1)) * DM + 64 * cw + 8 * (c >> 1));
    auto kgroup = [&](int g) {
      float x[16];
#pragma unroll
      for (int sub = 0; sub < 4; ++sub) {
        wait_landed(ctl, (unsigned)(8 * (16 * g + 4 * sub + 3) + cw + 1), have);
        v4u d[4];
#pragma unroll
        for (int i = 0; i < 4; ++i) d[i] = rp[(4 * sub + i) * 512];
        wait_lgkm();
        asm volatile("" : "+v"(d[0]), "+v"(d[1]), "+v"(d[2]), "+v"(d[3]));
        ctl[8 + cw] = (unsigned)(16 * g + 4 * sub + 4);
#pragma unroll
        for (int i = 0; i < 4; ++i) x[4 * sub + i] = dot8(d[i], q);
      }
      const float s = treduce16(x, c); sc[g * 64] = s; mx = fmaxf(mx, s);
    };
    v4u Wo[16];
#pragma unroll 1
    for (int g = 1; g < 3; ++g) kgroup(g);
#pragma unroll
    for (int t = 0; t < 8; ++t) Wo[t] = wop[t * (2 * DM / 8)];
    __builtin_amdgcn_sched_barrier(0);
#pragma unroll 1
    for (int g = 3; g < 5; ++g) kgroup(g);
#pragma unroll
    for (int t = 8; t < 16; ++t) Wo[t] = wop[t * (2 * DM / 8)];
    __builtin_amdgcn_sched_barrier(0);
#pragma unroll 1
    for (int g = 5; g < 8; ++g) kgroup(g);
    LAS float* ml = (LAS float*)(L + L_ML);
    {
      const float m = wave_max(mx);
      if (lane == 0) ml[cw] = m;
      wait_lgkm();
      if (lane == 0) __hip_atomic_fetch_add((LAS unsigned*)(L + L_CTL) + 3, 1u, __ATOMIC_RELAXED, __HIP_MEMORY_SCOPE_WORKGROUP);
    }
    unsigned Wp[16][4];
    {
      const bool odd = (c & 1) != 0;
      const unsigned sel_lo = odd ? 0x01000504u : 0x05040100u, sel_hi = odd ? 0x03020706u : 0x07060302u;
#pragma unroll
      for (int i = 0; i < 16; ++i)
#pragma unroll
        for (int j = 0; j < 2; ++j) {
          const unsigned keep = odd ? Wo[i][2 + j] : Wo[i][j], send = odd ? Wo[i][j] : Wo[i][2 + j];
          const unsigned recv = (unsigned)__builtin_amdgcn_mov_dpp((int)send, XOR1, 0xf, 0xf, true);
          Wp[i][2 * j] = __builtin_amdgcn_perm(recv, keep, sel_lo);
          Wp[i][2 * j + 1] = __builtin_amdgcn_perm(recv, keep, sel_hi);
          asm volatile("" : "+v"(Wp[i][2 * j]), "+v"(Wp[i][2 * j + 1]));
        }
    }
    for (unsigned spins = 0; spins < SPIN_CAP; ++spins) { if (__builtin_amdgcn_readfirstlane(ctl[3]) >= (unsigned)NCONS) break; }
    asm volatile("" ::: "memory");
    {
      const f32x4 m0 = *(const LAS f32x4*)ml, m1 = *(const LAS f32x4*)(ml + 4);
      const float M = fmaxf(fmaxf(fmaxf(m0[0], m0[1]), fmaxf(m0[2], m0[3])), fmaxf(fmaxf(m1[0], m1[1]), fmaxf(m1[2], m1[3])));
      const float mc = M * SM_C;
      float lsum = 0.f;
#pragma unroll
      for (int g = 0; g < 8; ++g) { const float p = __builtin_amdgcn_exp2f(fmaf(sc[g * 64], SM_C, -mc)); sc[g * 64] = p; lsum += p; }
      lsum = wave_sum(lsum);
      if (lane == 0) ml[8 + cw] = lsum;
    }
    f32x2 acc2[4];
#pragma unroll
    for (int j = 0; j < 4; ++j) acc2[j] = f32x2{0.f, 0.f};
    auto vgroup = [&](int g) {
      const float p = sc[g * 64];
      Unroll<0, 4>::run([&](auto S) {
        constexpr int sub = decltype(S)::value;
        wait_landed(ctl, (unsigned)(1024 + 8 * (16 * g + 4 * sub + 3) + cw + 1), have);
        v4u d[4];
#pragma unroll
        for (int i = 0; i < 4; ++i) d[i] = rp[(4 * sub + i) * 512];
        wait_lgkm();
        asm volatile("" : "+v"(d[0]), "+v"(d[1]), "+v"(d[2]), "+v"(d[3]));
        ctl[8 + cw] = (unsigned)(128 + 16 * g + 4 * sub + 4);
        pv_step<4 * sub + 0>(acc2, d[0], p); pv_step<4 * sub + 1>(acc2, d[1], p); pv_step<4 * sub + 2>(acc2, d[2], p); pv_step<4 * sub + 3>(acc2, d[3], p);
        asm volatile("" : "+v"(acc2[0]), "+v"(acc2[1]), "+v"(acc2[2]), "+v"(acc2[3]));
      });
    };
#pragma unroll 1
    for (int g = 0; g < 7; ++g) vgroup(g);
    for (unsigned spins = 0; spins < SPIN_CAP; ++spins) { if (__builtin_amdgcn_readfirstlane(ctl[4]) != 0u) break; __builtin_amdgcn_s_sleep(1); }
    asm volatile("" ::: "memory");
    const int col = 64 * cw + 4 * c + r;
    float ymoe = ((const LAS float*)(L + L_YMOE))[col];
    float gate = ((const LAS float*)(L + L_SCAL))[0];
    unsigned tag32 = f2u(((const LAS float*)(L + L_SCAL))[2]);
    GAS u64* dst = (GAS u64*)(ws + WS_INBOX) + ((size_t)(b * NH + (col >> 4)) * NH + h) * 16 + (col & 15);
    asm volatile("" : "+v"(ymoe), "+v"(gate), "+v"(tag32), "+v"(dst));
    vgroup(7);
    {
      float u[4];
#pragma unroll
      for (int j = 0; j < 2; ++j)
#pragma unroll
        for (int e = 0; e < 2; ++e) {
          auto t = __builtin_amdgcn_permlane32_swap(f2u(acc2[j][e]), f2u(acc2[j + 2][e]), false, false);
          u[2 * j + e] = u2f(t[0]) + u2f(t[1]);
        }
      f32x2 o2;
#pragma unroll
      for (int e = 0; e < 2; ++e) {
        auto t = __builtin_amdgcn_permlane16_swap(f2u(u[e]), f2u(u[2 + e]), false, false);
        o2[e] = u2f(t[0]) + u2f(t[1]);
      }
      *(LAS f32x2*)(L + L_QPART + (cw * 128 + 8 * c + 2 * r) * 4) = o2;
    }
    raw_barrier();
    if (cw == 0) { STAMP(4); STAMP_OUT(4); }
    {
      f32x2 o2 = {0.f, 0.f};
#pragma unroll
      for (int ww = 0; ww < 8; ++ww) o2 += *(const LAS f32x2*)(L + L_QPART + (ww * 128 + 2 * lane) * 4);
      const f32x4 l0 = *(const LAS f32x4*)(ml + 8), l1 = *(const LAS f32x4*)(ml + 12);
      const float inv = 1.0f / (((l0[0] + l0[1]) + (l0[2] + l0[3])) + ((l1[0] + l1[1]) + (l1[2] + l1[3])));
      const float op = u2f(pack_bf16(o2[0] * inv, o2[1] * inv));
      float y[4] = {0.f, 0.f, 0.f, 0.f};
      Unroll<0, 16>::run([&](auto I) {
        constexpr int i = decltype(I)::value;
        const unsigned os = f2u(dpp<0x150 + i>(op));
#pragma unroll
        for (int j = 0; j < 4; ++j) y[j] = dot2(os, Wp[i][j], y[j]);
      });
      auto t0 = __builtin_amdgcn_permlane32_swap(f2u(y[0]), f2u(y[2]), false, false);
      auto t1 = __builtin_amdgcn_permlane32_swap(f2u(y[1]), f2u(y[3]), false, false);
      const float u0 = u2f(t0[0]) + u2f(t0[1]), u1 = u2f(t1[0]) + u2f(t1[1]);
      auto t2 = __builtin_amdgcn_permlane16_swap(f2u(u0), f2u(u1), false, false);
      const float ycol = u2f(t2[0]) + u2f(t2[1]);
      __hip_atomic_store(dst, ((u64)tag32 << 32) | f2u(fmaf(ycol, gate, ymoe)), RLX_AGENT);
      if (cw == 0) { STAMP(5); STAMP_OUT(5); }
    }
    return;
  } else if (w == NCONS + 2) {
    asm volatile("s_barrier" ::: "memory");
    const unsigned tagb = ((unsigned)__hip_atomic_load((GAS u64*)(ws + WS_EPOCH), RLX_AGENT) + 1u) & 0xffffu;
    GAS unsigned* board = (GAS unsigned*)(ws + WS_PROG);
    for (unsigned it = 0; it < 4096; ++it) {
      __builtin_amdgcn_s_sleep(GOV_SLEEP);
      const unsigned my = __builtin_amdgcn_readfirstlane(ctl[0]);
      if (my >= (unsigned)(NPIECE - 192)) break;
      const unsigned me = (my - (unsigned)((__builtin_amdgcn_s_memrealtime() * 287ull) >> 10)) & 0xffffu;
      if (lane == 0) __hip_atomic_store(board + bid * 32, (tagb << 16) | me, RLX_AGENT);
      float sum = 0.f, cnt = 0.f;
      if (lane < NPEER) {
        const unsigned ej = __hip_atomic_load(board + ((bid + 1 + 33 * lane + 7 * it) & 255) * 32, RLX_AGENT);
        const bool ok = (ej >> 16) == tagb; sum = ok ? (float)(short)(u16)(me - ej) : 0.f; cnt = ok ? 1.f : 0.f;
      }
      sum = row_sum(sum); cnt = row_sum(cnt);
      const float diff = u2f(__builtin_amdgcn_readfirstlane(f2u(sum / fmaxf(cnt, 1.f))));
      cnt = u2f(__builtin_amdgcn_readfirstlane(f2u(cnt)));
      const unsigned cls = (cnt < (float)(NPEER / 2)) ? 0u : (diff >= (float)TH2) ? 1u : (diff >= (float)TH1) ? 2u : (diff <= (float)-TH2) ? 4u : (diff <= (float)-TH1) ? 3u : 0u;
      if (lane == 0) ctl[2] = cls;
    }
    raw_barrier();
  } else {
    asm volatile("s_barrier" ::: "memory");
    GAS u64* epoch = (GAS u64*)(ws + WS_EPOCH) + b * 16;
    const u64 e0 = __hip_atomic_load(epoch, RLX_AGENT);
    const GAS v4u* xp = (const GAS v4u*)(xg + b * DM + 8 * lane);
    const v4u xv = xp[0];
    const v4u gw = *(const GAS v4u*)(gate_w + 8 * lane);
    const v4u wr = *(const GAS v4u*)(w_r + 8 * lane);
    const u64 tag64 = e0 + 1ull;
    const unsigned tag32 = (unsigned)tag64 ^ (unsigned)(tag64 >> 32);
    {
      const __amdgpu_buffer_rsrc_t rsI = __builtin_amdgcn_make_buffer_rsrc((void*)(ws + WS_INBOX + (size_t)(b * NH + h) * 4096), 0, 4096, 0x00020000);
      const v4u inv = {0u, ~tag32, 0u, ~tag32};
#pragma unroll
      for (int i = 0; i < 4; ++i) __builtin_amdgcn_raw_buffer_store_b128(inv, rsI, lane * 16 + i * 1024, 0, 16);
      wait_vm<0>();
      if (lane == 0) __hip_atomic_store((GAS u64*)(ws + WS_READY) + b * NH + h, tag64, RLX_AGENT);
    }
    LAS float* xs = (LAS float*)(L + L_XS);
    { LAS f32x4* xs4 = (LAS f32x4*)xs + 2 * lane;
      xs4[0] = f32x4{bflo(xv.x), bfhi(xv.x), bflo(xv.y), bfhi(xv.y)}; xs4[1] = f32x4{bflo(xv.z), bfhi(xv.z), bflo(xv.w), bfhi(xv.w)}; }
    float sg = 0.f, sr = 0.f;
#pragma unroll
    for (int j = 0; j < 4; ++j) {
      sg += rbf(bflo(xv[j]) * bflo(gw[j])); sg += rbf(bfhi(xv[j]) * bfhi(gw[j]));
      sr = fmaf(bflo(xv[j]), bflo(wr[j]), sr); sr = fmaf(bfhi(xv[j]), bfhi(wr[j]), sr);
    }
    sg = rbf(wave_sum(sg)); sr = rbf(wave_sum(sr));
    const float g = rbf(sigmoidf_(sg)), rs = rbf(sigmoidf_(sr)), omg = rbf(1.0f - g);
    if (lane == 0) { LAS float* sca = (LAS float*)(L + L_SCAL); sca[0] = g; sca[1] = omg; sca[2] = u2f(tag32); ((LAS u64*)sca)[2] = tag64; }
    float y[8];
#pragma unroll
    for (int j = 0; j < 8; ++j) y[j] = 0.f;
    if (h < FF / FS) {
      for (unsigned spins = 0; spins < SPIN_CAP; ++spins) { if (__builtin_amdgcn_readfirstlane(ctl[1]) >= (unsigned)NCONS) break; __builtin_amdgcn_s_sleep(8); }
      const int rr = lane >> 3, cc = lane & 7;
      const GAS v4u* gp = (const GAS v4u*)(w_gate + (size_t)(64 * rr) * FF + h * FS + 8 * cc);
      const GAS v4u* up = (const GAS v4u*)(w_up + (size_t)(64 * rr) * FF + h * FS + 8 * cc);
      float ag[8], au[8];
#pragma unroll
      for (int j = 0; j < 8; ++j) { ag[j] = 0.f; au[j] = 0.f; }
#pragma unroll 1
      for (int it = 0; it < 16; ++it) {
        v4u Gc[4], Uc[4];
#pragma unroll
        for (int t = 0; t < 4; ++t) { Gc[t] = gp[(it * 4 + t) * (FF / 8)]; Uc[t] = up[(it * 4 + t) * (FF / 8)]; }
        const f32x4 xq = *(const LAS f32x4*)(xs + 64 * rr + 4 * it);
#pragma unroll
        for (int t = 0; t < 4; ++t) {
          const float xt = xq[t];
#pragma unroll
          for (int j = 0; j < 4; ++j) {
            ag[2 * j] = fmaf(xt, bflo(Gc[t][j]), ag[2 * j]); ag[2 * j + 1] = fmaf(xt, bfhi(Gc[t][j]), ag[2 * j + 1]);
            au[2 * j] = fmaf(xt, bflo(Uc[t][j]), au[2 * j]); au[2 * j + 1] = fmaf(xt, bfhi(Uc[t][j]), au[2 * j + 1]);
          }
        }
        __builtin_amdgcn_s_sleep(MOE_SLEEP);
      }
      LAS float* act = (LAS float*)(L + L_ACT);
#pragma unroll
      for (int j = 0; j < 8; ++j) {
        float gg = ag[j], uu = au[j];
        gg += dpp<XOR8>(gg); uu += dpp<XOR8>(uu);
        gg = rbf(xrow_sum(gg)); uu = rbf(xrow_sum(uu));
        const float si = rbf(gg * sigmoidf_(gg));
        if (lane < 8) act[8 * lane + j] = rbf(si * uu);
      }
      const GAS v4u* dp = (const GAS v4u*)(w_down + (size_t)(h * FS) * DM + lane * 8);
#pragma unroll 1
      for (int it = 0; it < 8; ++it) {
        v4u Dc[8];
#pragma unroll
        for (int t = 0; t < 8; ++t) Dc[t] = dp[(it * 8 + t) * (DM / 8)];
#pragma unroll
        for (int t = 0; t < 8; ++t) {
          const float a = act[it * 8 + t];
#pragma unroll
          for (int j = 0; j < 4; ++j) { y[2 * j] = fmaf(a, bflo(Dc[t][j]), y[2 * j]); y[2 * j + 1] = fmaf(a, bfhi(Dc[t][j]), y[2 * j + 1]); }
        }
        __builtin_amdgcn_s_sleep(MOE_SLEEP);
      }
    }
    const float f = omg * rs;
    LAS f32x4* ym = (LAS f32x4*)(L + L_YMOE) + 2 * lane;
    ym[0] = f32x4{y[0] * f, y[1] * f, y[2] * f, y[3] * f}; ym[1] = f32x4{y[4] * f, y[5] * f, y[6] * f, y[7] * f};
    {
      GAS u64* rdy = (GAS u64*)(ws + WS_READY) + b * NH + (lane & 31);
      for (unsigned spins = 0; spins < SPIN_CAP; ++spins) {
        const u64 v = __hip_atomic_load(rdy, RLX_AGENT);
        if (__all(v == tag64)) break;
        __builtin_amdgcn_s_sleep(8);
      }
    }
    wait_lgkm();
    if (lane == 0) ctl[4] = 1u;
    STAMP(9); STAMP_OUT(9);
    raw_barrier();
  }
  {
    const unsigned tag32 = f2u(((const LAS float*)(L + L_SCAL))[2]);
    const float omg = ((const LAS float*)(L + L_SCAL))[1];
    GAS u64* src = (GAS u64*)(ws + WS_INBOX) + ((size_t)(b * NH + h) * NH + 8 * r) * 16 + c;
    const float xres = bflo((unsigned)xg[b * DM + 16 * h + c]);
    if (w == NCONS + 1) __builtin_amdgcn_s_sleep(POLL_STAG); else if (w == NCONS + 2) __builtin_amdgcn_s_sleep(2 * POLL_STAG);
    unsigned v[8];
    for (unsigned spins = 0; spins < SPIN_CAP; ++spins) {
      bool ok = true;
#pragma unroll
      for (int i = 0; i < 8; ++i) { const u64 x = __hip_atomic_load(src + i * 16, RLX_AGENT); v[i] = (unsigned)x; ok &= (unsigned)(x >> 32) == tag32; }
      if (__all(ok)) break;
    }
    float s = 0.f;
#pragma unroll
    for (int i = 0; i < 8; ++i) s += u2f(v[i]);
    s = xrow_sum(s);
    s = fmaf(omg, xres, s);
    const unsigned o2 = pack_bf16(s, dpp<XOR1>(s));
    if (lane < 16 && (lane & 1) == 0) *(GAS unsigned*)(out + b * DM + 16 * h + lane) = o2;
    if (w == 0 && h == 0 && lane == 0) __hip_atomic_store((GAS u64*)(ws + WS_EPOCH) + b * 16, ((const LAS u64*)(L + L_SCAL))[2], RLX_AGENT);
    if (LEAK != 0 && w == 0 && bid == LEAK_BID) {
      wait_vm<0>();
      const u64 now = __builtin_amdgcn_s_memrealtime();
      if (LEAK == 1) lk_x = now;
      long long extra = 10ll * ((long long)(lk_x - lk_t0) - (long long)LEAK_OFF);
      if (extra < 0) extra = 0;
      if (extra > 40000) extra = 40000;
      while ((long long)(__builtin_amdgcn_s_memrealtime() - now) < extra) __builtin_amdgcn_s_sleep(1);
    }
  }
}
}

extern "C" void kernel_launch(void* const* d_in, const int* in_sizes, int n_in, void* d_out, int out_size, void* d_ws, size_t ws_size, hipStream_t stream) {
  (void)in_sizes; (void)n_in; (void)out_size; (void)ws_size;
  (void)hipFuncSetAttribute((const void*)fused_kernel, hipFuncAttributeMaxDynamicSharedMemorySize, LDS_BYTES);
  fused_kernel<<<dim3(NB * NH), dim3(NTHR), LDS_BYTES, stream>>>(
      (const u16*)d_in[0], (const u16*)d_in[1], (const u16*)d_in[2], (const u16*)d_in[3], (const u16*)d_in[4],
      (const u16*)d_in[6], (const u16*)d_in[7], (const u16*)d_in[8], (const u16*)d_in[9], (const u16*)d_in[10], (u16*)d_out, (unsigned char*)d_ws);
}
```

```cpp
#include <hip/hip_runtime.h>
#include <stdint.h>
#include <type_traits>

#define GAS __attribute__((address_space(1)))
#define LAS __attribute__((address_space(3)))
typedef unsigned short u16;
typedef unsigned long long u64;
typedef unsigned v4u __attribute__((ext_vector_type(4)));
typedef unsigned v2u __attribute__((ext_vector_type(2)));
typedef float f32x4 __attribute__((ext_vector_type(4)));
typedef float f32x2 __attribute__((ext_vector_type(2)));
typedef __bf16 bf16x2 __attribute__((ext_vector_type(2)));

#define GOV_SCALAR 1
#define STAMP(i)
#define STAMP_OUT(i)
#define INFL 24
#define W_M2 22
#define W_M1 24
#define W_P1 32
#define W_P2 44
#define TH1 10
#define TH2 28
#define BLK 32
#define POLL_STAG 4
#define KW_ADD 0
#define KW_END 1024
#define WO_G1 (1 + (h % 3))
#define WO_G2 (4 + (h % 3) + ((h & 4) >> 2))
#define GOV_LATE 1500
#define GOV_SLEEP2 30
#define GOV_EXIT 72
#define GOV_RATE 283ull
#define GOV_ALPHA 0.5f
#define TH1L 6
#define TH2L 16
#define NPEER 8
#define GOV_SLEEP 100
#define WQ_FIRST 4
#define WQ_STEP 4
#define MOE_SLEEP 32
#define BOOT 32
#define BOOT_END 256

namespace {
constexpr int NB = 8, NH = 32, SK = 4096, DH = 128, DM = 512, FF = 1792, FS = 64;
constexpr int NCONS = 8, NWAVES = NCONS + 3, NTHR = 64 * NWAVES;
constexpr float SM_C = 0.088388347648318440f * 1.4426950408889634f;
constexpr int NPIECE = 2048;
constexpr int RING = 128;
constexpr unsigned SPIN_CAP = 1u << 22;

constexpr int L_RING = 0;
constexpr int L_SC = RING * 1024;
constexpr int L_QPART = L_SC + 8 * 512 * 4;
constexpr int L_ML = L_QPART + 8 * 128 * 4;
constexpr int L_CTL = L_ML + 64;
constexpr int L_SCAL = L_CTL + 64;
constexpr int L_ACT = L_SCAL + 64;
constexpr int L_YMOE = L_ACT + 256;
constexpr int L_XS = L_YMOE + 512 * 4;
constexpr int LDS_BYTES = L_XS + 512 * 4;
static_assert(LDS_BYTES <= 160 * 1024, "lds");

constexpr size_t WS_EPOCH = 0;
constexpr size_t WS_READY = 4096;
constexpr size_t WS_PROG = 32768;
constexpr size_t WS_INBOX = 65536;

#define RLX_AGENT __ATOMIC_RELAXED, __HIP_MEMORY_SCOPE_AGENT

__device__ __forceinline__ float u2f(unsigned u) { return __builtin_bit_cast(float, u); }
__device__ __forceinline__ unsigned f2u(float f) { return __builtin_bit_cast(unsigned, f); }
__device__ __forceinline__ float bflo(unsigned u) { return u2f(u << 16); }
__device__ __forceinline__ float bfhi(unsigned u) { return u2f(u & 0xffff0000u); }
__device__ __forceinline__ float dot2(unsigned a, unsigned b, float acc) {
  return __builtin_amdgcn_fdot2_f32_bf16(__builtin_bit_cast(bf16x2, a), __builtin_bit_cast(bf16x2, b), acc, false);
}
__device__ __forceinline__ unsigned pack_bf16(float lo, float hi) {
  bf16x2 v = {(__bf16)lo, (__bf16)hi};
  return __builtin_bit_cast(unsigned, v);
}
__device__ __forceinline__ float rbf(float x) { return u2f((unsigned)__builtin_bit_cast(u16, (__bf16)x) << 16); }
template <int CTRL> __device__ __forceinline__ float dpp(float x) {
  return __builtin_bit_cast(float, __builtin_amdgcn_mov_dpp(__builtin_bit_cast(int, x), CTRL, 0xf, 0xf, true));
}
constexpr int XOR1 = 0xB1, XOR2 = 0x4E, XOR7 = 0x141, XOR8 = 0x128;
__device__ __forceinline__ float xrow_sum(float x) {
  auto s = __builtin_amdgcn_permlane16_swap(f2u(x), f2u(x), false, false);
  x = u2f(s[0]) + u2f(s[1]);
  auto t = __builtin_amdgcn_permlane32_swap(f2u(x), f2u(x), false, false);
  return u2f(t[0]) + u2f(t[1]);
}
__device__ __forceinline__ float xrow_max(float x) {
  auto s = __builtin_amdgcn_permlane16_swap(f2u(x), f2u(x), false, false);
  x = fmaxf(u2f(s[0]), u2f(s[1]));
  auto t = __builtin_amdgcn_permlane32_swap(f2u(x), f2u(x), false, false);
  return fmaxf(u2f(t[0]), u2f(t[1]));
}
__device__ __forceinline__ float row_sum(float x) { x += dpp<XOR8>(x); x += dpp<XOR7>(x); x += dpp<XOR2>(x); x += dpp<XOR1>(x); return x; }
__device__ __forceinline__ float row_max(float x) { x = fmaxf(x, dpp<XOR8>(x)); x = fmaxf(x, dpp<XOR7>(x)); x = fmaxf(x, dpp<XOR2>(x)); x = fmaxf(x, dpp<XOR1>(x)); return x; }
__device__ __forceinline__ float wave_sum(float x) { return xrow_sum(row_sum(x)); }
__device__ __forceinline__ float wave_max(float x) { return xrow_max(row_max(x)); }
__device__ __forceinline__ float dot8(const v4u k, const v4u q) {
  float d = dot2(k.x, q.x, 0.f); d = dot2(k.y, q.y, d); d = dot2(k.z, q.z, d); d = dot2(k.w, q.w, d); return d;
}
__device__ __forceinline__ float treduce16(float (&x)[16], int c) {
#define TR_STEP(HALF, CTRL, BIT) _Pragma("unroll") for (int h = 0; h < (HALF); ++h) { \
    const float lo_ = x[h], hi_ = x[h + (HALF)]; const float t1_ = lo_ + dpp<CTRL>(lo_), t2_ = hi_ + dpp<CTRL>(hi_); x[h] = (c & (BIT)) ? t2_ : t1_; }
  TR_STEP(8, XOR8, 8) TR_STEP(4, XOR7, 4) TR_STEP(2, XOR2, 2) TR_STEP(1, XOR1, 1)
#undef TR_STEP
  return x[0];
}
__device__ __forceinline__ float sigmoidf_(float v) { return 1.0f / (1.0f + __expf(-v)); }
template <int T> __device__ __forceinline__ void pv_step(f32x2 (&acc)[4], const v4u v, float p) {
  const float pb = dpp<0x150 + T>(p);
  const f32x2 p2 = {pb, pb};
#pragma unroll
  for (int j = 0; j < 4; ++j) acc[j] = __builtin_elementwise_fma(p2, f32x2{bflo(v[j]), bfhi(v[j])}, acc[j]);
}
template <int T, int N> struct Unroll { template <class F> static __device__ __forceinline__ void run(F&& f) { f(std::integral_constant<int, T>{}); Unroll<T + 1, N>::run(f); } };
template <int N> struct Unroll<N, N> { template <class F> static __device__ __forceinline__ void run(F&&) {} };
template <int N> __device__ __forceinline__ void wait_vm() { asm volatile("s_waitcnt vmcnt(%0)" ::"n"(N) : "memory"); }
__device__ __forceinline__ void wait_lgkm() { asm volatile("s_waitcnt lgkmcnt(0)" ::: "memory"); }
__device__ __forceinline__ void raw_barrier() { asm volatile("s_waitcnt lgkmcnt(0)\n\ts_barrier" ::: "memory"); }

__device__ __forceinline__ void wait_landed(volatile LAS unsigned* ctl, unsigned need, unsigned& have) {
  if (have >= need) return;
  for (unsigned spins = 0; spins < SPIN_CAP; ++spins) {
    have = __builtin_amdgcn_readfirstlane(ctl[0]);
    if (have >= need) break;
    __builtin_amdgcn_s_sleep(1);
  }
  asm volatile("" ::: "memory");
}

__device__ __forceinline__ int min_consumed(volatile LAS unsigned* ctl, int lane) {
  unsigned cn = ctl[8 + (lane & 7)];
  cn = min(cn, (unsigned)__builtin_amdgcn_mov_dpp((int)cn, XOR1, 0xf, 0xf, true));
  cn = min(cn, (unsigned)__builtin_amdgcn_mov_dpp((int)cn, XOR2, 0xf, 0xf, true));
  cn = min(cn, (unsigned)__builtin_amdgcn_mov_dpp((int)cn, XOR7, 0xf, 0xf, true));
  return (int)__builtin_amdgcn_readfirstlane(cn);
}

__global__ void __launch_bounds__(NTHR) fused_kernel(const u16* __restrict__ xg, const u16* __restrict__ Kg, const u16* __restrict__ Vg, const u16* __restrict__ wq,
                                                     const u16* __restrict__ wo, const u16* __restrict__ w_r, const u16* __restrict__ w_gate, const u16* __restrict__ w_up,
                                                     const u16* __restrict__ w_down, const u16* __restrict__ gate_w, u16* __restrict__ out, unsigned char* __restrict__ ws) {
  extern __shared__ __attribute__((aligned(16))) unsigned char smem[];
  LAS unsigned char* L = (LAS unsigned char*)smem;
  const int tid = threadIdx.x, lane = tid & 63, w = __builtin_amdgcn_readfirstlane(tid >> 6);
  const int bid = blockIdx.x;
  const int h = (bid & 7) * 4 + (bid >> 6), b = (bid >> 3) & 7;
  const int r = lane >> 4, c = lane & 15;
  volatile LAS unsigned* ctl = (volatile LAS unsigned*)(L + L_CTL);
  STAMP(0);

  if (__builtin_expect(w == 0, 1)) {
    __builtin_amdgcn_s_setprio(3);
    const size_t kvoff = (size_t)(b * NH + h) * SK * DH;
    const __amdgpu_buffer_rsrc_t rsK = __builtin_amdgcn_make_buffer_rsrc((void*)(Kg + kvoff), 0, SK * DH * 2, 0x00020000);
    const __amdgpu_buffer_rsrc_t rsV = __builtin_amdgcn_make_buffer_rsrc((void*)(Vg + kvoff), 0, SK * DH * 2, 0x00020000);
    const int voff = lane * 16;
    if (lane < 16) ctl[lane] = 0u;
#pragma unroll
    for (int i = 0; i < INFL; ++i) __builtin_amdgcn_raw_ptr_buffer_load_lds(rsK, (LAS void*)(L + L_RING + i * 1024), 16, voff, i * 1024, 0, 2);
    STAMP(10);
    raw_barrier();
    STAMP(11);
    int free_until = RING;
    auto make_room = [&](int gp) {
      if (gp + 4 > free_until) {
        free_until = 8 * min_consumed(ctl, lane) + RING;
        if (gp + 4 > free_until) {
          wait_vm<0>();
          ctl[0] = (unsigned)gp;
          for (unsigned spins = 0; spins < SPIN_CAP; ++spins) {
            free_until = 8 * min_consumed(ctl, lane) + RING;
            if (gp + 4 <= free_until) break;
            __builtin_amdgcn_s_sleep(1);
          }
        }
        asm volatile("" ::: "memory");
      }
    };
    for (int p = INFL; p < BOOT_END; p += 4) {
      make_room(p);
      LAS unsigned char* dst = L + L_RING + (p & (RING - 1)) * 1024;
#pragma unroll
      for (int i = 0; i < 4; ++i) __builtin_amdgcn_raw_ptr_buffer_load_lds(rsK, (LAS void*)(dst + i * 1024), 16, voff, (p + i) * 1024, 0, 2);
      wait_vm<BOOT>();
      if (p + 4 > BOOT) ctl[0] = (unsigned)(p + 4 - BOOT);
    }
    int pub = BOOT_END - BOOT;
    auto run_block = [&](auto WW, const __amdgpu_buffer_rsrc_t rs, int goff, int p0, int p1) {
      constexpr int W = decltype(WW)::value;
#pragma unroll 1
      for (int p = p0; p < p1; p += 4) {
        const int gp = goff + p;
        make_room(gp);
        LAS unsigned char* dst = L + L_RING + (gp & (RING - 1)) * 1024;
#pragma unroll
        for (int i = 0; i < 4; ++i) __builtin_amdgcn_raw_ptr_buffer_load_lds(rs, (LAS void*)(dst + i * 1024), 16, voff, (p + i) * 1024, 0, 2);
        wait_vm<W>();
        pub = max(pub, gp + 4 - W);
        ctl[0] = (unsigned)pub;
      }
    };
    for (int half = 0; half < 2; ++half) {
      const __amdgpu_buffer_rsrc_t rs = half ? rsV : rsK;
#pragma unroll 1
      for (int p = half ? 0 : BOOT_END; p < 1024; p += BLK) {
        const int cl = __builtin_amdgcn_readfirstlane(ctl[2]);
        if (half == 0 && p < KW_END) {
          if (cl <= 2) run_block(std::integral_constant<int, INFL + KW_ADD>{}, rs, 0, p, p + BLK);
          else if (cl == 3) run_block(std::integral_constant<int, W_P1 + KW_ADD>{}, rs, 0, p, p + BLK);
          else run_block(std::integral_constant<int, W_P2 + KW_ADD>{}, rs, 0, p, p + BLK);
        }
        else if (cl == 0) run_block(std::integral_constant<int, INFL>{}, rs, half * 1024, p, p + BLK);
        else if (cl == 1) run_block(std::integral_constant<int, W_M2>{}, rs, half * 1024, p, p + BLK);
        else if (cl == 2) run_block(std::integral_constant<int, W_M1>{}, rs, half * 1024, p, p + BLK);
        else if (cl == 3) run_block(std::integral_constant<int, W_P1>{}, rs, half * 1024, p, p + BLK);
        else run_block(std::integral_constant<int, W_P2>{}, rs, half * 1024, p, p + BLK);
      }
      if (half == 0) { STAMP(2); }
    }
    wait_vm<0>();
    ctl[0] = (unsigned)NPIECE;
    STAMP(3);
    STAMP_OUT(0); STAMP_OUT(2); STAMP_OUT(3); STAMP_OUT(10); STAMP_OUT(11); STAMP_OUT(12);
    raw_barrier();
  } else if (w <= NCONS) {
    const int cw = w - 1;
    asm volatile("s_barrier" ::: "memory");
    const GAS v4u* xp = (const GAS v4u*)(xg + b * DM + 64 * cw + 16 * r);
    const v4u xa = xp[0], xb = xp[1];
    const GAS v4u* wqp = (const GAS v4u*)(wq + (size_t)(64 * cw + 16 * r) * (NH * DH) + h * DH + 8 * c);
    v4u W[16];
    STAMP(6);
    const LAS v4u* rp = (const LAS v4u*)(L + L_RING + cw * 1024) + lane;
    unsigned have = 0;
    v4u P[16];
#pragma unroll
    for (int t = 0; t < WQ_FIRST; ++t) W[t] = wqp[(size_t)t * (NH * DH / 8)];
    __builtin_amdgcn_sched_barrier(0);
#pragma unroll
    for (int sub = 0; sub < 4; ++sub) {
      wait_landed(ctl, (unsigned)(8 * (4 * sub + 3) + cw + 1), have);
#pragma unroll
      for (int i = 0; i < 4; ++i) P[4 * sub + i] = rp[(4 * sub + i) * 512];
      wait_lgkm();
      asm volatile("" : "+v"(P[4 * sub]), "+v"(P[4 * sub + 1]), "+v"(P[4 * sub + 2]), "+v"(P[4 * sub + 3]));
      ctl[8 + cw] = (unsigned)(4 * sub + 4);
#pragma unroll
      for (int t = WQ_FIRST + sub * WQ_STEP; t < WQ_FIRST + (sub + 1) * WQ_STEP && t < 16; ++t) W[t] = wqp[(size_t)t * (NH * DH / 8)];
      __builtin_amdgcn_sched_barrier(0);
    }
    STAMP(7);
    __builtin_amdgcn_sched_barrier(0);
    float qa[8];
#pragma unroll
    for (int j = 0; j < 8; ++j) qa[j] = 0.f;
#pragma unroll
    for (int t = 0; t < 16; ++t) {
      const unsigned xw = (t < 8) ? xa[t >> 1] : xb[(t - 8) >> 1];
      const float xv = (t & 1) ? bfhi(xw) : bflo(xw);
#pragma unroll
      for (int j = 0; j < 4; ++j) { qa[2 * j] = fmaf(xv, bflo(W[t][j]), qa[2 * j]); qa[2 * j + 1] = fmaf(xv, bfhi(W[t][j]), qa[2 * j + 1]); }
    }
#pragma unroll
    for (int j = 0; j < 8; ++j) qa[j] = xrow_sum(qa[j]);
    if (r == 0) {
      LAS f32x4* qp = (LAS f32x4*)(L + L_QPART) + cw * 32 + c * 2;
      qp[0] = f32x4{qa[0], qa[1], qa[2], qa[3]}; qp[1] = f32x4{qa[4], qa[5], qa[6], qa[7]};
    }
    wait_lgkm();
    if (lane == 0) __hip_atomic_fetch_add((LAS unsigned*)(L + L_CTL) + 1, 1u, __ATOMIC_RELAXED, __HIP_MEMORY_SCOPE_WORKGROUP);
    for (unsigned spins = 0; spins < SPIN_CAP; ++spins) { if (__builtin_amdgcn_readfirstlane(ctl[1]) >= (unsigned)NCONS) break; }
    asm volatile("" ::: "memory");
    v4u q;
    {
      f32x4 s0 = {0.f, 0.f, 0.f, 0.f}, s1 = {0.f, 0.f, 0.f, 0.f};
#pragma unroll
      for (int ww = 0; ww < 8; ++ww) { const LAS f32x4* qp = (const LAS f32x4*)(L + L_QPART) + ww * 32 + c * 2; s0 += qp[0]; s1 += qp[1]; }
      q = v4u{pack_bf16(s0[0], s0[1]), pack_bf16(s0[2], s0[3]), pack_bf16(s1[0], s1[1]), pack_bf16(s1[2], s1[3])};
    }
    if (cw == 0) { STAMP(1); STAMP_OUT(1); STAMP_OUT(6); STAMP_OUT(7); }
    LAS float* sc = (LAS float*)(L + L_SC) + cw * 512 + lane;
    float mx;
    {
      float x[16];
#pragma unroll
      for (int t = 0; t < 16; ++t) x[t] = dot8(P[t], q);
      const float s = treduce16(x, c); sc[0] = s; mx = s;
    }
    const GAS v4u* wop = (const GAS v4u*)(wo + (size_t)(h * DH + 32 * r + (c & 1)) * DM + 64 * cw + 8 * (c >> 1));
    auto kgroup = [&](int g) {
      float x[16];
#pragma unroll
      for (int sub = 0; sub < 4; ++sub) {
        wait_landed(ctl, (unsigned)(8 * (16 * g + 4 * sub + 3) + cw + 1), have);
        v4u d[4];
#pragma unroll
        for (int i = 0; i < 4; ++i) d[i] = rp[(4 * sub + i) * 512];
        wait_lgkm();
        asm volatile("" : "+v"(d[0]), "+v"(d[1]), "+v"(d[2]), "+v"(d[3]));
        ctl[8 + cw] = (unsigned)(16 * g + 4 * sub + 4);
#pragma unroll
        for (int i = 0; i < 4; ++i) x[4 * sub + i] = dot8(d[i], q);
      }
      const float s = treduce16(x, c); sc[g * 64] = s; mx = fmaxf(mx, s);
    };
    v4u Wo[16];
    const int g1 = WO_G1, g2 = WO_G2;
#pragma unroll 1
    for (int g = 1; g < g1; ++g) kgroup(g);
#pragma unroll
    for (int t = 0; t < 8; ++t) Wo[t] = wop[t * (2 * DM / 8)];
    __builtin_amdgcn_sched_barrier(0);
#pragma unroll 1
    for (int g = g1; g < g2; ++g) kgroup(g);
#pragma unroll
    for (int t = 8; t < 16; ++t) Wo[t] = wop[t * (2 * DM / 8)];
    __builtin_amdgcn_sched_barrier(0);
#pragma unroll 1
    for (int g = g2; g < 8; ++g) kgroup(g);
    LAS float* ml = (LAS float*)(L + L_ML);
    {
      const float m = wave_max(mx);
      if (lane == 0) ml[cw] = m;
      wait_lgkm();
      if (lane == 0) __hip_atomic_fetch_add((LAS unsigned*)(L + L_CTL) + 3, 1u, __ATOMIC_RELAXED, __HIP_MEMORY_SCOPE_WORKGROUP);
    }
    unsigned Wp[16][4];
    {
      const bool odd = (c & 1) != 0;
      const unsigned sel_lo = odd ? 0x01000504u : 0x05040100u, sel_hi = odd ? 0x03020706u : 0x07060302u;
#pragma unroll
      for (int i = 0; i < 16; ++i)
#pragma unroll
        for (int j = 0; j < 2; ++j) {
          const unsigned keep = odd ? Wo[i][2 + j] : Wo[i][j], send = odd ? Wo[i][j] : Wo[i][2 + j];
          const unsigned recv = (unsigned)__builtin_amdgcn_mov_dpp((int)send, XOR1, 0xf, 0xf, true);
          Wp[i][2 * j] = __builtin_amdgcn_perm(recv, keep, sel_lo);
          Wp[i][2 * j + 1] = __builtin_amdgcn_perm(recv, keep, sel_hi);
          asm volatile("" : "+v"(Wp[i][2 * j]), "+v"(Wp[i][2 * j + 1]));
        }
    }
    for (unsigned spins = 0; spins < SPIN_CAP; ++spins) { if (__builtin_amdgcn_readfirstlane(ctl[3]) >= (unsigned)NCONS) break; }
    asm volatile("" ::: "memory");
    {
      const f32x4 m0 = *(const LAS f32x4*)ml, m1 = *(const LAS f32x4*)(ml + 4);
      const float M = fmaxf(fmaxf(fmaxf(m0[0], m0[1]), fmaxf(m0[2], m0[3])), fmaxf(fmaxf(m1[0], m1[1]), fmaxf(m1[2], m1[3])));
      const float mc = M * SM_C;
      float lsum = 0.f;
#pragma unroll
      for (int g = 0; g < 8; ++g) { const float p = __builtin_amdgcn_exp2f(fmaf(sc[g * 64], SM_C, -mc)); sc[g * 64] = p; lsum += p; }
      lsum = wave_sum(lsum);
      if (lane == 0) ml[8 + cw] = lsum;
    }
    f32x2 acc2[4];
#pragma unroll
    for (int j = 0; j < 4; ++j) acc2[j] = f32x2{0.f, 0.f};
    auto vgroup = [&](int g) {
      const float p = sc[g * 64];
      Unroll<0, 4>::run([&](auto S) {
        constexpr int sub = decltype(S)::value;
        wait_landed(ctl, (unsigned)(1024 + 8 * (16 * g + 4 * sub + 3) + cw + 1), have);
        v4u d[4];
#pragma unroll
        for (int i = 0; i < 4; ++i) d[i] = rp[(4 * sub + i) * 512];
        wait_lgkm();
        asm volatile("" : "+v"(d[0]), "+v"(d[1]), "+v"(d[2]), "+v"(d[3]));
        ctl[8 + cw] = (unsigned)(128 + 16 * g + 4 * sub + 4);
        pv_step<4 * sub + 0>(acc2, d[0], p); pv_step<4 * sub + 1>(acc2, d[1], p); pv_step<4 * sub + 2>(acc2, d[2], p); pv_step<4 * sub + 3>(acc2, d[3], p);
        asm volatile("" : "+v"(acc2[0]), "+v"(acc2[1]), "+v"(acc2[2]), "+v"(acc2[3]));
      });
    };
#pragma unroll 1
    for (int g = 0; g < 7; ++g) vgroup(g);
    for (unsigned spins = 0; spins < SPIN_CAP; ++spins) { if (__builtin_amdgcn_readfirstlane(ctl[4]) != 0u) break; __builtin_amdgcn_s_sleep(1); }
    asm volatile("" ::: "memory");
    const int col = 64 * cw + 4 * c + r;
    float ymoe = ((const LAS float*)(L + L_YMOE))[col];
    float gate = ((const LAS float*)(L + L_SCAL))[0];
    unsigned tag32 = f2u(((const LAS float*)(L + L_SCAL))[2]);
    GAS u64* dst = (GAS u64*)(ws + WS_INBOX) + ((size_t)(b * NH + (col >> 4)) * NH + h) * 16 + (col & 15);
    asm volatile("" : "+v"(ymoe), "+v"(gate), "+v"(tag32), "+v"(dst));
    vgroup(7);
    {
      float u[4];
#pragma unroll
      for (int j = 0; j < 2; ++j)
#pragma unroll
        for (int e = 0; e < 2; ++e) {
          auto t = __builtin_amdgcn_permlane32_swap(f2u(acc2[j][e]), f2u(acc2[j + 2][e]), false, false);
          u[2 * j + e] = u2f(t[0]) + u2f(t[1]);
        }
      f32x2 o2;
#pragma unroll
      for (int e = 0; e < 2; ++e) {
        auto t = __builtin_amdgcn_permlane16_swap(f2u(u[e]), f2u(u[2 + e]), false, false);
        o2[e] = u2f(t[0]) + u2f(t[1]);
      }
      *(LAS f32x2*)(L + L_QPART + (cw * 128 + 8 * c + 2 * r) * 4) = o2;
    }
    raw_barrier();
    if (cw == 0) { STAMP(4); STAMP_OUT(4); }
    {
      f32x2 o2 = {0.f, 0.f};
#pragma unroll
      for (int ww = 0; ww < 8; ++ww) o2 += *(const LAS f32x2*)(L + L_QPART + (ww * 128 + 2 * lane) * 4);
      const f32x4 l0 = *(const LAS f32x4*)(ml + 8), l1 = *(const LAS f32x4*)(ml + 12);
      const float inv = 1.0f / (((l0[0] + l0[1]) + (l0[2] + l0[3])) + ((l1[0] + l1[1]) + (l1[2] + l1[3])));
      const float op = u2f(pack_bf16(o2[0] * inv, o2[1] * inv));
      float y[4] = {0.f, 0.f, 0.f, 0.f};
      Unroll<0, 16>::run([&](auto I) {
        constexpr int i = decltype(I)::value;
        const unsigned os = f2u(dpp<0x150 + i>(op));
#pragma unroll
        for (int j = 0; j < 4; ++j) y[j] = dot2(os, Wp[i][j], y[j]);
      });
      auto t0 = __builtin_amdgcn_permlane32_swap(f2u(y[0]), f2u(y[2]), false, false);
      auto t1 = __builtin_amdgcn_permlane32_swap(f2u(y[1]), f2u(y[3]), false, false);
      const float u0 = u2f(t0[0]) + u2f(t0[1]), u1 = u2f(t1[0]) + u2f(t1[1]);
      auto t2 = __builtin_amdgcn_permlane16_swap(f2u(u0), f2u(u1), false, false);
      const float ycol = u2f(t2[0]) + u2f(t2[1]);
      __hip_atomic_store(dst, ((u64)tag32 << 32) | f2u(fmaf(ycol, gate, ymoe)), RLX_AGENT);
      if (cw == 0) { STAMP(5); STAMP_OUT(5); }
    }
    return;
  } else if (w == NCONS + 2) {
    asm volatile("s_barrier" ::: "memory");
    const unsigned tagb = ((unsigned)__hip_atomic_load((GAS u64*)(ws + WS_EPOCH), RLX_AGENT) + 1u) & 0xffffu;
    GAS unsigned* board = (GAS unsigned*)(ws + WS_PROG);
    float gavg = 0.f;
    unsigned me_prev = 0; bool have_avg = false;
    unsigned last = 0;
    for (unsigned it = 0; it < 8192; ++it) {
      if (last < (unsigned)GOV_LATE) __builtin_amdgcn_s_sleep(GOV_SLEEP); else __builtin_amdgcn_s_sleep(GOV_SLEEP2);
      const unsigned my = __builtin_amdgcn_readfirstlane(ctl[0]);
      last = my;
      if (my >= (unsigned)(NPIECE - GOV_EXIT)) break;
      const unsigned me = (my - (unsigned)((__builtin_amdgcn_s_memrealtime() * GOV_RATE) >> 10)) & 0xffffu;
      if (lane == 0) __hip_atomic_store(board + bid * 32, (tagb << 16) | me, RLX_AGENT);
      unsigned ev[NPEER];
      {
        const unsigned char* bb = (const unsigned char*)(ws + WS_PROG);
#pragma unroll
        for (int j = 0; j < NPEER; ++j) {
          const unsigned off = (unsigned)__builtin_amdgcn_readfirstlane(((bid + 1 + 33 * j + 7 * (int)it) & 255) * 128);
          asm volatile("s_load_dword %0, %1, %2 glc" : "=&s"(ev[j]) : "s"(bb), "s"(off) : "memory");
        }
        asm volatile("s_waitcnt lgkmcnt(0)" ::: "memory");
#pragma unroll
        for (int j = 0; j < NPEER; ++j) asm volatile("" : "+s"(ev[j]));
      }
      float sum = 0.f, cnt = 0.f;
#pragma unroll
      for (int j = 0; j < NPEER; ++j) { const bool ok = (ev[j] >> 16) == tagb; sum += ok ? (float)(short)(u16)(ev[j] - me) : 0.f; cnt += ok ? 1.f : 0.f; }
      const float smp = sum / fmaxf(cnt, 1.f);
      if (cnt >= (float)(NPEER / 2)) {
        const float prev = have_avg ? gavg - (float)(short)(u16)(me - me_prev) : smp;
        gavg = prev + (float)GOV_ALPHA * (smp - prev);
        me_prev = me; have_avg = true;
        const float diff = -gavg;
        const bool late = my >= (unsigned)GOV_LATE;
        const float t1 = late ? (float)TH1L : (float)TH1, t2 = late ? (float)TH2L : (float)TH2;
        const unsigned cls = (diff >= t2) ? 1u : (diff >= t1) ? 2u : (diff <= -t2) ? 4u : (diff <= -t1) ? 3u : 0u;
        if (lane == 0) ctl[2] = cls;
      }
    }
    raw_barrier();
  } else {
    asm volatile("s_barrier" ::: "memory");
    GAS u64* epoch = (GAS u64*)(ws + WS_EPOCH) + b * 16;
    const u64 e0 = __hip_atomic_load(epoch, RLX_AGENT);
    const GAS v4u* xp = (const GAS v4u*)(xg + b * DM + 8 * lane);
    const v4u xv = xp[0];
    const v4u gw = *(const GAS v4u*)(gate_w + 8 * lane);
    const v4u wr = *(const GAS v4u*)(w_r + 8 * lane);
    const u64 tag64 = e0 + 1ull;
    const unsigned tag32 = (unsigned)tag64 ^ (unsigned)(tag64 >> 32);
    {
      const __amdgpu_buffer_rsrc_t rsI = __builtin_amdgcn_make_buffer_rsrc((void*)(ws + WS_INBOX + (size_t)(b * NH + h) * 4096), 0, 4096, 0x00020000);
      const v4u inv = {0u, ~tag32, 0u, ~tag32};
#pragma unroll
      for (int i = 0; i < 4; ++i) __builtin_amdgcn_raw_buffer_store_b128(inv, rsI, lane * 16 + i * 1024, 0, 16);
      wait_vm<0>();
      if (lane == 0) __hip_atomic_store((GAS u64*)(ws + WS_READY) + b * NH + h, tag64, RLX_AGENT);
    }
    LAS float* xs = (LAS float*)(L + L_XS);
    { LAS f32x4* xs4 = (LAS f32x4*)xs + 2 * lane;
      xs4[0] = f32x4{bflo(xv.x), bfhi(xv.x), bflo(xv.y), bfhi(xv.y)}; xs4[1] = f32x4{bflo(xv.z), bfhi(xv.z), bflo(xv.w), bfhi(xv.w)}; }
    float sg = 0.f, sr = 0.f;
#pragma unroll
    for (int j = 0; j < 4; ++j) {
      sg += rbf(bflo(xv[j]) * bflo(gw[j])); sg += rbf(bfhi(xv[j]) * bfhi(gw[j]));
      sr = fmaf(bflo(xv[j]), bflo(wr[j]), sr); sr = fmaf(bfhi(xv[j]), bfhi(wr[j]), sr);
    }
    sg = rbf(wave_sum(sg)); sr = rbf(wave_sum(sr));
    const float g = rbf(sigmoidf_(sg)), rs = rbf(sigmoidf_(sr)), omg = rbf(1.0f - g);
    if (lane == 0) { LAS float* sca = (LAS float*)(L + L_SCAL); sca[0] = g; sca[1] = omg; sca[2] = u2f(tag32); ((LAS u64*)sca)[2] = tag64; }
    float y[8];
#pragma unroll
    for (int j = 0; j < 8; ++j) y[j] = 0.f;
    if (h < FF / FS) {
      for (unsigned spins = 0; spins < SPIN_CAP; ++spins) { if (__builtin_amdgcn_readfirstlane(ctl[1]) >= (unsigned)NCONS) break; __builtin_amdgcn_s_sleep(8); }
      const int rr = lane >> 3, cc = lane & 7;
      const GAS v4u* gp = (const GAS v4u*)(w_gate + (size_t)(64 * rr) * FF + h * FS + 8 * cc);
      const GAS v4u* up = (const GAS v4u*)(w_up + (size_t)(64 * rr) * FF + h * FS + 8 * cc);
      float ag[8], au[8];
#pragma unroll
      for (int j = 0; j < 8; ++j) { ag[j] = 0.f; au[j] = 0.f; }
#pragma unroll 1
      for (int it = 0; it < 16; ++it) {
        v4u Gc[4], Uc[4];
#pragma unroll
        for (int t = 0; t < 4; ++t) { Gc[t] = gp[(it * 4 + t) * (FF / 8)]; Uc[t] = up[(it * 4 + t) * (FF / 8)]; }
        const f32x4 xq = *(const LAS f32x4*)(xs + 64 * rr + 4 * it);
#pragma unroll
        for (int t = 0; t < 4; ++t) {
          const float xt = xq[t];
#pragma unroll
          for (int j = 0; j < 4; ++j) {
            ag[2 * j] = fmaf(xt, bflo(Gc[t][j]), ag[2 * j]); ag[2 * j + 1] = fmaf(xt, bfhi(Gc[t][j]), ag[2 * j + 1]);
            au[2 * j] = fmaf(xt, bflo(Uc[t][j]), au[2 * j]); au[2 * j + 1] = fmaf(xt, bfhi(Uc[t][j]), au[2 * j + 1]);
          }
        }
        __builtin_amdgcn_s_sleep(MOE_SLEEP);
      }
      LAS float* act = (LAS float*)(L + L_ACT);
#pragma unroll
      for (int j = 0; j < 8; ++j) {
        float gg = ag[j], uu = au[j];
        gg += dpp<XOR8>(gg); uu += dpp<XOR8>(uu);
        gg = rbf(xrow_sum(gg)); uu = rbf(xrow_sum(uu));
        const float si = rbf(gg * sigmoidf_(gg));
        if (lane < 8) act[8 * lane + j] = rbf(si * uu);
      }
      const GAS v4u* dp = (const GAS v4u*)(w_down + (size_t)(h * FS) * DM + lane * 8);
#pragma unroll 1
      for (int it = 0; it < 8; ++it) {
        v4u Dc[8];
#pragma unroll
        for (int t = 0; t < 8; ++t) Dc[t] = dp[(it * 8 + t) * (DM / 8)];
#pragma unroll
        for (int t = 0; t < 8; ++t) {
          const float a = act[it * 8 + t];
#pragma unroll
          for (int j = 0; j < 4; ++j) { y[2 * j] = fmaf(a, bflo(Dc[t][j]), y[2 * j]); y[2 * j + 1] = fmaf(a, bfhi(Dc[t][j]), y[2 * j + 1]); }
        }
        __builtin_amdgcn_s_sleep(MOE_SLEEP);
      }
    }
    const float f = omg * rs;
    LAS f32x4* ym = (LAS f32x4*)(L + L_YMOE) + 2 * lane;
    ym[0] = f32x4{y[0] * f, y[1] * f, y[2] * f, y[3] * f}; ym[1] = f32x4{y[4] * f, y[5] * f, y[6] * f, y[7] * f};
    {
      GAS u64* rdy = (GAS u64*)(ws + WS_READY) + b * NH + (lane & 31);
      for (unsigned spins = 0; spins < SPIN_CAP; ++spins) {
        const u64 v = __hip_atomic_load(rdy, RLX_AGENT);
        if (__all(v == tag64)) break;
        __builtin_amdgcn_s_sleep(8);
      }
    }
    wait_lgkm();
    if (lane == 0) ctl[4] = 1u;
    STAMP(9); STAMP_OUT(9);
    raw_barrier();
  }
  {
    const unsigned tag32 = f2u(((const LAS float*)(L + L_SCAL))[2]);
    const float omg = ((const LAS float*)(L + L_SCAL))[1];
    GAS u64* src = (GAS u64*)(ws + WS_INBOX) + ((size_t)(b * NH + h) * NH + 8 * r) * 16 + c;
    const float xres = bflo((unsigned)xg[b * DM + 16 * h + c]);
    if (w == NCONS + 1) __builtin_amdgcn_s_sleep(POLL_STAG); else if (w == NCONS + 2) __builtin_amdgcn_s_sleep(2 * POLL_STAG);
    unsigned v[8];
    for (unsigned spins = 0; spins < SPIN_CAP; ++spins) {
      bool ok = true;
#pragma unroll
      for (int i = 0; i < 8; ++i) { const u64 x = __hip_atomic_load(src + i * 16, RLX_AGENT); v[i] = (unsigned)x; ok &= (unsigned)(x >> 32) == tag32; }
      if (__all(ok)) break;
    }
    float s = 0.f;
#pragma unroll
    for (int i = 0; i < 8; ++i) s += u2f(v[i]);
    s = xrow_sum(s);
    s = fmaf(omg, xres, s);
    const unsigned o2 = pack_bf16(s, dpp<XOR1>(s));
    if (lane < 16 && (lane & 1) == 0) *(GAS unsigned*)(out + b * DM + 16 * h + lane) = o2;
    if (w == 0 && h == 0 && lane == 0) __hip_atomic_store((GAS u64*)(ws + WS_EPOCH) + b * 16, ((const LAS u64*)(L + L_SCAL))[2], RLX_AGENT);
  }
}
}

extern "C" void kernel_launch(void* const* d_in, const int* in_sizes, int n_in, void* d_out, int out_size, void* d_ws, size_t ws_size, hipStream_t stream) {
  (void)in_sizes; (void)n_in; (void)out_size; (void)ws_size;
  (void)hipFuncSetAttribute((const void*)fused_kernel, hipFuncAttributeMaxDynamicSharedMemorySize, LDS_BYTES);
  fused_kernel<<<dim3(NB * NH), dim3(NTHR), LDS_BYTES, stream>>>(
      (const u16*)d_in[0], (const u16*)d_in[1], (const u16*)d_in[2], (const u16*)d_in[3], (const u16*)d_in[4],
      (const u16*)d_in[6], (const u16*)d_in[7], (const u16*)d_in[8], (const u16*)d_in[9], (const u16*)d_in[10], (u16*)d_out, (unsigned char*)d_ws);
}
```

```cpp
#define LEAK 0
#define LEAK_OFF 0
#define LEAK_BID 9
#include <hip/hip_runtime.h>
#include <stdint.h>
#include <type_traits>

#define GAS __attribute__((address_space(1)))
#define LAS __attribute__((address_space(3)))
typedef unsigned short u16;
typedef unsigned long long u64;
typedef unsigned v4u __attribute__((ext_vector_type(4)));
typedef unsigned v2u __attribute__((ext_vector_type(2)));
typedef float f32x4 __attribute__((ext_vector_type(4)));
typedef float f32x2 __attribute__((ext_vector_type(2)));
typedef __bf16 bf16x2 __attribute__((ext_vector_type(2)));
typedef __bf16 bf16x8 __attribute__((ext_vector_type(8)));

namespace {
constexpr int NB = 8, NH = 32, SK = 4096, DH = 128, DM = 512, FF = 1792, FS = 64;
constexpr int NCONS = 8, NWAVES = NCONS + 3, NTHR = 64 * NWAVES;
constexpr float SM_C = 0.088388347648318440f * 1.4426950408889634f;
constexpr int NPIECE = 2048;
constexpr int RING = 128;
constexpr unsigned SPIN_CAP = 1u << 21;
constexpr int INFL = 24, W_M2 = 22, W_M1 = 24, W_P1 = 32, W_P2 = 44;
constexpr int FIRST = 24;
constexpr int BLK = 32;
constexpr int TH1 = 10, TH2 = 28, TH1L = 6, TH2L = 16, GOV_LATE = 1500, GOV_EXIT = 128, GOV_SLEEP = 100, GOV_SLEEP2 = 30, NPEER = 8;
constexpr unsigned long long GOV_RATE = 283ull;
constexpr float GOV_ALPHA = 0.5f;
constexpr int Q_DELAY = 4, Q_POLL = 2, POLL_DELAY = 6, POLL_STAG = 4, MOE_SLEEP = 20;
static_assert(FIRST % 4 == 0 && BLK % 4 == 0 && 1024 % BLK == 0, "the loader works in groups of four pieces that never straddle the ring's end");

constexpr int L_RING = 0;
constexpr int L_SC = RING * 1024;
constexpr int L_QPART = L_SC + 8 * 512 * 4;
constexpr int L_ML = L_QPART + 8 * 128 * 4;
constexpr int L_CTL = L_ML + 64;
constexpr int L_SCAL = L_CTL + 64;
constexpr int L_ACT = L_SCAL + 64;
constexpr int L_YMOE = L_ACT + 256;
constexpr int L_XS = L_YMOE + 512 * 4;
constexpr int LDS_BYTES = L_XS + 512 * 4;
static_assert(LDS_BYTES <= 160 * 1024, "lds");

constexpr size_t WS_EPOCH = 0;
constexpr size_t WS_READY = 4096;
constexpr size_t WS_PROG = 32768;
constexpr size_t WS_INBOX = 65536;
constexpr size_t WS_QIN = (size_t)2 << 20;

#define RLX_AGENT __ATOMIC_RELAXED, __HIP_MEMORY_SCOPE_AGENT

__device__ __forceinline__ float u2f(unsigned u) { return __builtin_bit_cast(float, u); }
__device__ __forceinline__ unsigned f2u(float f) { return __builtin_bit_cast(unsigned, f); }
__device__ __forceinline__ float bflo(unsigned u) { return u2f(u << 16); }
__device__ __forceinline__ float bfhi(unsigned u) { return u2f(u & 0xffff0000u); }
__device__ __forceinline__ float dot2(unsigned a, unsigned b, float acc) {
  return __builtin_amdgcn_fdot2_f32_bf16(__builtin_bit_cast(bf16x2, a), __builtin_bit_cast(bf16x2, b), acc, false);
}
__device__ __forceinline__ f32x4 mfma16(v4u a, v4u b, f32x4 c) {
  return __builtin_amdgcn_mfma_f32_16x16x32_bf16(__builtin_bit_cast(bf16x8, a), __builtin_bit_cast(bf16x8, b), c, 0, 0, 0);
}
__device__ __forceinline__ unsigned pack_bf16(float lo, float hi) {
  bf16x2 v = {(__bf16)lo, (__bf16)hi};
  return __builtin_bit_cast(unsigned, v);
}
__device__ __forceinline__ float rbf(float x) { return u2f((unsigned)__builtin_bit_cast(u16, (__bf16)x) << 16); }
template <int CTRL> __device__ __forceinline__ unsigned dppu(unsigned x) { return (unsigned)__builtin_amdgcn_mov_dpp((int)x, CTRL, 0xf, 0xf, true); }
template <int CTRL> __device__ __forceinline__ float dpp(float x) { return u2f(dppu<CTRL>(f2u(x))); }
constexpr int XOR1 = 0xB1, XOR2 = 0x4E, XOR7 = 0x141, XOR8 = 0x128, BCAST16 = 0x150;
__device__ __forceinline__ float xrow_sum(float x) {
  auto s = __builtin_amdgcn_permlane16_swap(f2u(x), f2u(x), false, false);
  x = u2f(s[0]) + u2f(s[1]);
  auto t = __builtin_amdgcn_permlane32_swap(f2u(x), f2u(x), false, false);
  return u2f(t[0]) + u2f(t[1]);
}
__device__ __forceinline__ float xrow_max(float x) {
  auto s = __builtin_amdgcn_permlane16_swap(f2u(x), f2u(x), false, false);
  x = fmaxf(u2f(s[0]), u2f(s[1]));
  auto t = __builtin_amdgcn_permlane32_swap(f2u(x), f2u(x), false, false);
  return fmaxf(u2f(t[0]), u2f(t[1]));
}
__device__ __forceinline__ float row_sum(float x) { x += dpp<XOR8>(x); x += dpp<XOR7>(x); x += dpp<XOR2>(x); x += dpp<XOR1>(x); return x; }
__device__ __forceinline__ float row_max(float x) { x = fmaxf(x, dpp<XOR8>(x)); x = fmaxf(x, dpp<XOR7>(x)); x = fmaxf(x, dpp<XOR2>(x)); x = fmaxf(x, dpp<XOR1>(x)); return x; }
__device__ __forceinline__ float wave_sum(float x) { return xrow_sum(row_sum(x)); }
__device__ __forceinline__ float wave_max(float x) { return xrow_max(row_max(x)); }
__device__ __forceinline__ float dot8(const v4u k, const v4u q) {
  float d = dot2(k.x, q.x, 0.f); d = dot2(k.y, q.y, d); d = dot2(k.z, q.z, d); d = dot2(k.w, q.w, d); return d;
}
__device__ __forceinline__ float treduce16(float (&x)[16], int c) {
#define TR_STEP(HALF, CTRL, BIT) _Pragma("unroll") for (int h = 0; h < (HALF); ++h) { \
    const float lo_ = x[h], hi_ = x[h + (HALF)]; const float t1_ = lo_ + dpp<CTRL>(lo_), t2_ = hi_ + dpp<CTRL>(hi_); x[h] = (c & (BIT)) ? t2_ : t1_; }
  TR_STEP(8, XOR8, 8) TR_STEP(4, XOR7, 4) TR_STEP(2, XOR2, 2) TR_STEP(1, XOR1, 1)
#undef TR_STEP
  return x[0];
}
__device__ __forceinline__ float sigmoidf_(float v) { return 1.0f / (1.0f + __expf(-v)); }
template <int T> __device__ __forceinline__ void pv_step(f32x2 (&acc)[4], const v4u v, float p) {
  const float pb = dpp<BCAST16 + T>(p);
  const f32x2 p2 = {pb, pb};
#pragma unroll
  for (int j = 0; j < 4; ++j) acc[j] = __builtin_elementwise_fma(p2, f32x2{bflo(v[j]), bfhi(v[j])}, acc[j]);
}
template <int T, int N> struct Unroll { template <class F> static __device__ __forceinline__ void run(F&& f) { f(std::integral_constant<int, T>{}); Unroll<T + 1, N>::run(f); } };
template <int N> struct Unroll<N, N> { template <class F> static __device__ __forceinline__ void run(F&&) {} };
template <int N> __device__ __forceinline__ void wait_vm() { asm volatile("s_waitcnt vmcnt(%0)" ::"n"(N) : "memory"); }
__device__ __forceinline__ void wait_lgkm() { asm volatile("s_waitcnt lgkmcnt(0)" ::: "memory"); }
__device__ __forceinline__ void raw_barrier() { asm volatile("s_waitcnt lgkmcnt(0)\n\ts_barrier" ::: "memory"); }
template <int SLEEP> __device__ __forceinline__ void wait_ge(volatile LAS unsigned* word, unsigned need) {
  for (unsigned spins = 0; spins < SPIN_CAP; ++spins) {
    if ((unsigned)__builtin_amdgcn_readfirstlane(*word) >= need) break;
    if (SLEEP) __builtin_amdgcn_s_sleep(SLEEP);
  }
  asm volatile("" ::: "memory");
}
__device__ __forceinline__ void wait_landed(volatile LAS unsigned* ctl, unsigned need, unsigned& have) {
  if (have >= need) return;
  for (unsigned spins = 0; spins < SPIN_CAP; ++spins) {
    have = __builtin_amdgcn_readfirstlane(ctl[0]);
    if (have >= need) break;
    __builtin_amdgcn_s_sleep(1);
  }
  asm volatile("" ::: "memory");
}
__device__ __forceinline__ int min_consumed(volatile LAS unsigned* ctl, int lane) {
  unsigned cn = ctl[8 + (lane & 7)];
  cn = min(cn, dppu<XOR1>(cn)); cn = min(cn, dppu<XOR2>(cn)); cn = min(cn, dppu<XOR7>(cn));
  return (int)__builtin_amdgcn_readfirstlane(cn);
}
__device__ __forceinline__ unsigned tag32_of(u64 tag64) { return (unsigned)tag64 ^ (unsigned)(tag64 >> 32); }

__global__ void __launch_bounds__(NTHR, 3) fused_kernel(const u16* __restrict__ xg, const u16* __restrict__ Kg, const u16* __restrict__ Vg, const u16* __restrict__ wq,
                                                     const u16* __restrict__ wo, const u16* __restrict__ w_r, const u16* __restrict__ w_gate, const u16* __restrict__ w_up,
                                                     const u16* __restrict__ w_down, const u16* __restrict__ gate_w, u16* __restrict__ out, unsigned char* __restrict__ ws) {
  extern __shared__ __attribute__((aligned(16))) unsigned char smem[];
  LAS unsigned char* L = (LAS unsigned char*)smem;
  const int tid = threadIdx.x, lane = tid & 63, w = __builtin_amdgcn_readfirstlane(tid >> 6);
  const int bid = blockIdx.x;
  const int h = (bid & 7) * 4 + (bid >> 6), b = (bid >> 3) & 7;
  const int r = lane >> 4, c = lane & 15;
  volatile LAS unsigned* ctl = (volatile LAS unsigned*)(L + L_CTL);
  const u64 lk_t0 = __builtin_amdgcn_s_memrealtime(); u64 lk_x = lk_t0;
  if (tid < 16) ctl[tid] = 0u;
  asm volatile("" ::"s"(Kg), "s"(Vg), "s"(wq), "s"(xg), "s"(ws));
  raw_barrier();

  if (__builtin_expect(w == 0, 1)) {
    __builtin_amdgcn_s_setprio(3);
    const size_t kvoff = (size_t)(b * NH + h) * SK * DH;
    const __amdgpu_buffer_rsrc_t rsK = __builtin_amdgcn_make_buffer_rsrc((void*)(Kg + kvoff), 0, SK * DH * 2, 0x00020000);
    const __amdgpu_buffer_rsrc_t rsV = __builtin_amdgcn_make_buffer_rsrc((void*)(Vg + kvoff), 0, SK * DH * 2, 0x00020000);
    const int voff = lane * 16;
#pragma unroll
    for (int i = 0; i < FIRST; ++i) __builtin_amdgcn_raw_ptr_buffer_load_lds(rsK, (LAS void*)(L + L_RING + i * 1024), 16, voff, i * 1024, 0, 2);
    int free_until = RING, pub = 0;
    auto make_room = [&](int gp) {
      if (gp + 4 > free_until) {
        free_until = 8 * min_consumed(ctl, lane) + RING;
        if (gp + 4 > free_until) {
          wait_vm<0>();
          pub = gp;
          ctl[0] = (unsigned)gp;
          for (unsigned spins = 0; spins < SPIN_CAP; ++spins) {
            free_until = 8 * min_consumed(ctl, lane) + RING;
            if (gp + 4 <= free_until) break;
            __builtin_amdgcn_s_sleep(1);
          }
        }
        asm volatile("" ::: "memory");
      }
    };
    auto run_block = [&](auto WW, const __amdgpu_buffer_rsrc_t rs, int goff, int p0, int p1) {
      constexpr int W = decltype(WW)::value;
#pragma unroll 1
      for (int p = p0; p < p1; p += 4) {
        const int gp = goff + p;
        make_room(gp);
        LAS unsigned char* dst = L + L_RING + (gp & (RING - 1)) * 1024;
#pragma unroll
        for (int i = 0; i < 4; ++i) __builtin_amdgcn_raw_ptr_buffer_load_lds(rs, (LAS void*)(dst + i * 1024), 16, voff, (p + i) * 1024, 0, 2);
        wait_vm<W>();
        pub = max(pub, gp + 4 - W);
        ctl[0] = (unsigned)pub;
      }
    };
    run_block(std::integral_constant<int, INFL>{}, rsK, 0, FIRST, ((FIRST + BLK - 1) / BLK) * BLK);
    for (int half = 0; half < 2; ++half) {
      const __amdgpu_buffer_rsrc_t rs = half ? rsV : rsK;
#pragma unroll 1
      for (int p = half ? 0 : ((FIRST + BLK - 1) / BLK) * BLK; p < 1024; p += BLK) {
        const int cl = __builtin_amdgcn_readfirstlane(ctl[2]);
        if (cl == 0) run_block(std::integral_constant<int, INFL>{}, rs, half * 1024, p, p + BLK);
        else if (cl == 1) run_block(std::integral_constant<int, W_M2>{}, rs, half * 1024, p, p + BLK);
        else if (cl == 2) run_block(std::integral_constant<int, W_M1>{}, rs, half * 1024, p, p + BLK);
        else if (cl == 3) run_block(std::integral_constant<int, W_P1>{}, rs, half * 1024, p, p + BLK);
        else run_block(std::integral_constant<int, W_P2>{}, rs, half * 1024, p, p + BLK);
        if (LEAK == 4 && half == 0 && p == 128) lk_x = __builtin_amdgcn_s_memrealtime();
      }
      if (LEAK == 2 && half == 0) lk_x = __builtin_amdgcn_s_memrealtime();
    }
    Unroll<0, 10>::run([&](auto I) {
      constexpr int i = decltype(I)::value;
      constexpr int n = i == 0 ? 32 : i == 1 ? 24 : i == 2 ? 18 : i == 3 ? 14 : i == 4 ? 10 : i == 5 ? 7 : i == 6 ? 4 : i == 7 ? 2 : i == 8 ? 1 : 0;
      wait_vm<n>();
      pub = max(pub, NPIECE - n);
      ctl[0] = (unsigned)pub;
    });
    if (LEAK == 5) lk_x = __builtin_amdgcn_s_memrealtime();
    raw_barrier();
  } else if (w <= NCONS) {
    const int cw = w - 1;
    v4u q;
    {
      wait_ge<1>(ctl + 6, 1u);
      __builtin_amdgcn_s_sleep(Q_DELAY);
      const u64 tag40 = ((const LAS u64*)(L + L_SCAL))[1] & 0xFFFFFFFFFFull;
      GAS u64* src = (GAS u64*)(ws + WS_QIN) + (size_t)((h * NB + b) * 8 + cw) * 128 + lane;
      u64 g0 = 0, g1 = 0;
      for (unsigned spins = 0; spins < SPIN_CAP; ++spins) {
        g0 = __hip_atomic_load(src, RLX_AGENT); g1 = __hip_atomic_load(src + 64, RLX_AGENT);
        if (__all((g0 >> 24) == tag40 && (g1 >> 24) == tag40)) break;
        __builtin_amdgcn_s_sleep(Q_POLL);
      }
      LAS float* qp = (LAS float*)(L + L_QPART) + cw * 128 + 8 * c + r;
      qp[0] = u2f((unsigned)g0 << 8); qp[4] = u2f((unsigned)g1 << 8);
      wait_lgkm();
      if (lane == 0) __hip_atomic_fetch_add((LAS unsigned*)(L + L_CTL) + 1, 1u, __ATOMIC_RELAXED, __HIP_MEMORY_SCOPE_WORKGROUP);
      wait_ge<0>(ctl + 1, (unsigned)NCONS);
      f32x4 s0 = {0.f, 0.f, 0.f, 0.f}, s1 = {0.f, 0.f, 0.f, 0.f};
#pragma unroll
      for (int ww = 0; ww < 8; ++ww) { const LAS f32x4* p4 = (const LAS f32x4*)(L + L_QPART) + ww * 32 + c * 2; s0 += p4[0]; s1 += p4[1]; }
      q = v4u{pack_bf16(s0[0], s0[1]), pack_bf16(s0[2], s0[3]), pack_bf16(s1[0], s1[1]), pack_bf16(s1[2], s1[3])};
    }
    if (LEAK == 3 && cw == 0 && lane == 0) ctl[7] = (unsigned)(__builtin_amdgcn_s_memrealtime() - lk_t0);
    LAS float* sc = (LAS float*)(L + L_SC) + cw * 512 + lane;
    const LAS v4u* rp = (const LAS v4u*)(L + L_RING + cw * 1024) + lane;
    unsigned have = 0;
    float mx = -INFINITY;
    auto kgroup = [&](int g) {
      float x[16];
#pragma unroll
      for (int sub = 0; sub < 4; ++sub) {
        wait_landed(ctl, (unsigned)(8 * (16 * g + 4 * sub + 3) + cw + 1), have);
        v4u d[4];
#pragma unroll
        for (int i = 0; i < 4; ++i) d[i] = rp[(4 * sub + i) * 512];
        wait_lgkm();
        asm volatile("" : "+v"(d[0]), "+v"(d[1]), "+v"(d[2]), "+v"(d[3]));
        ctl[8 + cw] = (unsigned)(16 * g + 4 * sub + 4);
#pragma unroll
        for (int i = 0; i < 4; ++i) x[4 * sub + i] = dot8(d[i], q);
      }
      const float s = treduce16(x, c); sc[g * 64] = s; mx = fmaxf(mx, s);
    };
    const GAS v4u* wop = (const GAS v4u*)(wo + (size_t)(h * DH + 32 * r + (c & 1)) * DM + 64 * cw + 8 * (c >> 1));
    v4u Wo[16];
    const int g1 = 1 + (h % 3), g2 = 4 + (h % 3) + ((h & 4) >> 2);
#pragma unroll 1
    for (int g = 0; g < g1; ++g) kgroup(g);
#pragma unroll
    for (int t = 0; t < 8; ++t) Wo[t] = wop[t * (2 * DM / 8)];
    __builtin_amdgcn_sched_barrier(0);
#pragma unroll 1
    for (int g = g1; g < g2; ++g) kgroup(g);
#pragma unroll
    for (int t = 8; t < 16; ++t) Wo[t] = wop[t * (2 * DM / 8)];
    __builtin_amdgcn_sched_barrier(0);
#pragma unroll 1
    for (int g = g2; g < 8; ++g) kgroup(g);
    LAS float* ml = (LAS float*)(L + L_ML);
    {
      const float m = wave_max(mx);
      if (lane == 0) ml[cw] = m;
      wait_lgkm();
      if (lane == 0) __hip_atomic_fetch_add((LAS unsigned*)(L + L_CTL) + 3, 1u, __ATOMIC_RELAXED, __HIP_MEMORY_SCOPE_WORKGROUP);
    }
    unsigned Wp[16][4];
    {
      const bool odd = (c & 1) != 0;
      const unsigned sel_lo = odd ? 0x01000504u : 0x05040100u, sel_hi = odd ? 0x03020706u : 0x07060302u;
#pragma unroll
      for (int i = 0; i < 16; ++i)
#pragma unroll
        for (int j = 0; j < 2; ++j) {
          const unsigned keep = odd ? Wo[i][2 + j] : Wo[i][j], send = odd ? Wo[i][j] : Wo[i][2 + j];
          const unsigned recv = dppu<XOR1>(send);
          Wp[i][2 * j] = __builtin_amdgcn_perm(recv, keep, sel_lo);
          Wp[i][2 * j + 1] = __builtin_amdgcn_perm(recv, keep, sel_hi);
          asm volatile("" : "+v"(Wp[i][2 * j]), "+v"(Wp[i][2 * j + 1]));
        }
    }
    wait_ge<0>(ctl + 3, (unsigned)NCONS);
    {
      const f32x4 m0 = *(const LAS f32x4*)ml, m1 = *(const LAS f32x4*)(ml + 4);
      const float M = fmaxf(fmaxf(fmaxf(m0[0], m0[1]), fmaxf(m0[2], m0[3])), fmaxf(fmaxf(m1[0], m1[1]), fmaxf(m1[2], m1[3])));
      const float mc = M * SM_C;
      float lsum = 0.f;
#pragma unroll
      for (int g = 0; g < 8; ++g) { const float p = __builtin_amdgcn_exp2f(fmaf(sc[g * 64], SM_C, -mc)); sc[g * 64] = p; lsum += p; }
      lsum = wave_sum(lsum);
      if (lane == 0) ml[8 + cw] = lsum;
    }
    f32x2 acc2[4];
#pragma unroll
    for (int j = 0; j < 4; ++j) acc2[j] = f32x2{0.f, 0.f};
    auto vbatch = [&](auto S, int g, float p) {
      constexpr int sub = decltype(S)::value;
      wait_landed(ctl, (unsigned)(1024 + 8 * (16 * g + 4 * sub + 3) + cw + 1), have);
      v4u d[4];
#pragma unroll
      for (int i = 0; i < 4; ++i) d[i] = rp[(4 * sub + i) * 512];
      wait_lgkm();
      asm volatile("" : "+v"(d[0]), "+v"(d[1]), "+v"(d[2]), "+v"(d[3]));
      ctl[8 + cw] = (unsigned)(128 + 16 * g + 4 * sub + 4);
      pv_step<4 * sub + 0>(acc2, d[0], p); pv_step<4 * sub + 1>(acc2, d[1], p); pv_step<4 * sub + 2>(acc2, d[2], p); pv_step<4 * sub + 3>(acc2, d[3], p);
      asm volatile("" : "+v"(acc2[0]), "+v"(acc2[1]), "+v"(acc2[2]), "+v"(acc2[3]));
    };
    float plast = 0.f;
#pragma unroll 1
    for (int g = 0; g < 8; ++g) {
      const float p = sc[g * 64];
      plast = p;
      vbatch(std::integral_constant<int, 0>{}, g, p); vbatch(std::integral_constant<int, 1>{}, g, p); vbatch(std::integral_constant<int, 2>{}, g, p);
      if (g < 7) vbatch(std::integral_constant<int, 3>{}, g, p);
    }
    wait_ge<1>(ctl + 4, 1u);
    const int col = 64 * cw + 4 * c + r;
    const float ymoe = ((const LAS float*)(L + L_YMOE))[col];
    const float gate = ((const LAS float*)(L + L_SCAL))[0];
    const unsigned tag32 = tag32_of(((const LAS u64*)(L + L_SCAL))[1]);
    GAS u64* dst = (GAS u64*)(ws + WS_INBOX) + ((size_t)(b * NH + (col >> 4)) * NH + h) * 16 + (col & 15);
    const f32x4 ls = *(const LAS f32x4*)(ml + 8), lt = *(const LAS f32x4*)(ml + 12);
    const float inv = 1.0f / (((ls[0] + ls[1]) + (ls[2] + ls[3])) + ((lt[0] + lt[1]) + (lt[2] + lt[3])));
    Unroll<12, 16>::run([&](auto T) {
      constexpr int t = decltype(T)::value;
      wait_landed(ctl, (unsigned)(1024 + 8 * (16 * 7 + t) + cw + 1), have);
      const v4u d = rp[t * 512];
      pv_step<t>(acc2, d, plast);
    });
    {
      float u[4];
#pragma unroll
      for (int j = 0; j < 2; ++j)
#pragma unroll
        for (int e = 0; e < 2; ++e) {
          auto t = __builtin_amdgcn_permlane32_swap(f2u(acc2[j][e]), f2u(acc2[j + 2][e]), false, false);
          u[2 * j + e] = u2f(t[0]) + u2f(t[1]);
        }
      f32x2 o2;
#pragma unroll
      for (int e = 0; e < 2; ++e) {
        auto t = __builtin_amdgcn_permlane16_swap(f2u(u[e]), f2u(u[2 + e]), false, false);
        o2[e] = u2f(t[0]) + u2f(t[1]);
      }
      *(LAS f32x2*)(L + L_QPART + (cw * 128 + 8 * c + 2 * r) * 4) = o2;
    }
    raw_barrier();
    {
      f32x2 o2 = {0.f, 0.f};
#pragma unroll
      for (int ww = 0; ww < 8; ++ww) o2 += *(const LAS f32x2*)(L + L_QPART + (ww * 128 + 2 * lane) * 4);
      const unsigned op = pack_bf16(o2[0] * inv, o2[1] * inv);
      f32x4 D[4];
#pragma unroll
      for (int j = 0; j < 4; ++j) D[j] = f32x4{0.f, 0.f, 0.f, 0.f};
      Unroll<0, 4>::run([&](auto S) {
        constexpr int s = decltype(S)::value;
        const v4u A = {dppu<BCAST16 + 4 * s>(op), dppu<BCAST16 + 4 * s + 1>(op), dppu<BCAST16 + 4 * s + 2>(op), dppu<BCAST16 + 4 * s + 3>(op)};
#pragma unroll
        for (int j = 0; j < 4; ++j) D[j] = mfma16(A, v4u{Wp[4 * s][j], Wp[4 * s + 1][j], Wp[4 * s + 2][j], Wp[4 * s + 3][j]}, D[j]);
      });
      const float d0 = D[0][0], d1 = D[1][0], d2 = D[2][0], d3 = D[3][0];
      const float ycol = (r & 2) ? ((r & 1) ? d3 : d2) : ((r & 1) ? d1 : d0);
      __hip_atomic_store(dst, ((u64)tag32 << 32) | f2u(fmaf(ycol, gate, ymoe)), RLX_AGENT);
    }
    return;
  } else if (w == NCONS + 2) {
    wait_ge<2>(ctl + 5, 1u);
    const unsigned tagb = (unsigned)__builtin_amdgcn_readfirstlane((unsigned)((const LAS u64*)(L + L_SCAL))[1]) & 0xffffu;
    GAS unsigned* board = (GAS unsigned*)(ws + WS_PROG);
    float gavg = 0.f;
    unsigned me_prev = 0, last = 0; bool have_avg = false;
    for (unsigned it = 0; it < 8192; ++it) {
      if (last < (unsigned)GOV_LATE) __builtin_amdgcn_s_sleep(GOV_SLEEP); else __builtin_amdgcn_s_sleep(GOV_SLEEP2);
      const unsigned my = __builtin_amdgcn_readfirstlane(ctl[0]);
      last = my;
      if (my >= (unsigned)(NPIECE - GOV_EXIT)) break;
      const unsigned me = (my - (unsigned)((__builtin_amdgcn_s_memrealtime() * GOV_RATE) >> 10)) & 0xffffu;
      if (lane == 0) __hip_atomic_store(board + bid * 32, (tagb << 16) | me, RLX_AGENT);
      unsigned ev[NPEER];
      {
        const unsigned char* bb = (const unsigned char*)(ws + WS_PROG);
#pragma unroll
        for (int j = 0; j < NPEER; ++j) {
          const unsigned off = (unsigned)__builtin_amdgcn_readfirstlane(((bid + 1 + 33 * j + 7 * (int)it) & 255) * 128);
          asm volatile("s_load_dword %0, %1, %2 glc" : "=&s"(ev[j]) : "s"(bb), "s"(off) : "memory");
        }
        asm volatile("s_waitcnt lgkmcnt(0)" ::: "memory");
#pragma unroll
        for (int j = 0; j < NPEER; ++j) asm volatile("" : "+s"(ev[j]));
      }
      float sum = 0.f, cnt = 0.f;
#pragma unroll
      for (int j = 0; j < NPEER; ++j) { const bool ok = (ev[j] >> 16) == tagb; sum += ok ? (float)(short)(u16)(ev[j] - me) : 0.f; cnt += ok ? 1.f : 0.f; }
      const float smp = sum / fmaxf(cnt, 1.f);
      if (cnt >= (float)(NPEER / 2)) {
        const float prev = have_avg ? gavg - (float)(short)(u16)(me - me_prev) : smp;
        gavg = prev + (float)GOV_ALPHA * (smp - prev);
        me_prev = me; have_avg = true;
        const float diff = -gavg;
        const bool late = my >= (unsigned)GOV_LATE;
        const float t1 = late ? (float)TH1L : (float)TH1, t2 = late ? (float)TH2L : (float)TH2;
        const unsigned cls = (diff >= t2) ? 1u : (diff >= t1) ? 2u : (diff <= -t2) ? 4u : (diff <= -t1) ? 3u : 0u;
        if (lane == 0) ctl[2] = cls;
      }
    }
    raw_barrier();
  } else {
    const u64 e0 = __hip_atomic_load((GAS u64*)(ws + WS_EPOCH), RLX_AGENT);
    const GAS v4u* xap = (const GAS v4u*)(xg + (lane & 7) * DM + 64 * b + 8 * r);
    const v4u xa0 = xap[0], xa1 = xap[4];
    const GAS v4u* wqp = (const GAS v4u*)(wq + (size_t)(64 * b + 8 * r) * (NH * DH) + h * DH + 8 * c);
    v4u W[16];
#pragma unroll
    for (int t = 0; t < 16; ++t) W[t] = wqp[(size_t)((t >> 3) * 32 + (t & 7)) * (NH * DH / 8)];
    __builtin_amdgcn_sched_barrier(0);
    const u64 tag64 = e0 + 1ull;
    if (lane == 0) { ((LAS u64*)(L + L_SCAL))[1] = tag64; wait_lgkm(); ctl[5] = 1u; }
    {
      f32x4 D[8];
#pragma unroll
      for (int j = 0; j < 8; ++j) D[j] = f32x4{0.f, 0.f, 0.f, 0.f};
#pragma unroll
      for (int u = 0; u < 2; ++u)
#pragma unroll
        for (int j = 0; j < 8; ++j) {
          const unsigned sel = (j & 1) ? 0x07060302u : 0x05040100u;
          v4u Bf;
#pragma unroll
          for (int qq = 0; qq < 4; ++qq) Bf[qq] = __builtin_amdgcn_perm(W[8 * u + 2 * qq + 1][j >> 1], W[8 * u + 2 * qq][j >> 1], sel);
          D[j] = mfma16(u ? xa1 : xa0, Bf, D[j]);
        }
      const u64 tg = (tag64 & 0xFFFFFFFFFFull) << 24;
      GAS u64* qdst = (GAS u64*)(ws + WS_QIN) + (size_t)((h * NB + 4 * (r & 1)) * 8 + b) * 128 + (r >> 1) * 64 + c;
#pragma unroll
      for (int i = 0; i < 4; ++i)
#pragma unroll
        for (int jj = 0; jj < 4; ++jj) {
          const float lo = D[jj][i], hi = D[jj + 4][i];
          __hip_atomic_store(qdst + (size_t)i * 8 * 128 + jj * 16, tg | (u64)(f2u((r & 2) ? hi : lo) >> 8), RLX_AGENT);
        }
    }
    if (lane == 0) ctl[6] = 1u;
    const GAS v4u* xp = (const GAS v4u*)(xg + b * DM + 8 * lane);
    const v4u xv = xp[0];
    const v4u gw = *(const GAS v4u*)(gate_w + 8 * lane);
    const v4u wr = *(const GAS v4u*)(w_r + 8 * lane);
    const unsigned tag32 = tag32_of(tag64);
    {
      const __amdgpu_buffer_rsrc_t rsI = __builtin_amdgcn_make_buffer_rsrc((void*)(ws + WS_INBOX + (size_t)(b * NH + h) * 4096), 0, 4096, 0x00020000);
      const v4u inv = {0u, ~tag32, 0u, ~tag32};
#pragma unroll
      for (int i = 0; i < 4; ++i) __builtin_amdgcn_raw_buffer_store_b128(inv, rsI, lane * 16 + i * 1024, 0, 16);
      wait_vm<0>();
      if (lane == 0) __hip_atomic_store((GAS u64*)(ws + WS_READY) + b * NH + h, tag64, RLX_AGENT);
    }
    LAS float* xs = (LAS float*)(L + L_XS);
    { LAS f32x4* xs4 = (LAS f32x4*)xs + 2 * lane;
      xs4[0] = f32x4{bflo(xv.x), bfhi(xv.x), bflo(xv.y), bfhi(xv.y)}; xs4[1] = f32x4{bflo(xv.z), bfhi(xv.z), bflo(xv.w), bfhi(xv.w)}; }
    float sg = 0.f, sr = 0.f;
#pragma unroll
    for (int j = 0; j < 4; ++j) {
      sg += rbf(bflo(xv[j]) * bflo(gw[j])); sg += rbf(bfhi(xv[j]) * bfhi(gw[j]));
      sr = fmaf(bflo(xv[j]), bflo(wr[j]), sr); sr = fmaf(bfhi(xv[j]), bfhi(wr[j]), sr);
    }
    sg = rbf(wave_sum(sg)); sr = rbf(wave_sum(sr));
    const float g = rbf(sigmoidf_(sg)), rs = rbf(sigmoidf_(sr)), omg = rbf(1.0f - g);
    if (lane == 0) { LAS float* sca = (LAS float*)(L + L_SCAL); sca[0] = g; sca[1] = omg; }
    float y[8];
#pragma unroll
    for (int j = 0; j < 8; ++j) y[j] = 0.f;
    if (h < FF / FS) {
      wait_ge<8>(ctl + 1, (unsigned)NCONS);
      const int rr = lane >> 3, cc = lane & 7;
      const GAS v4u* gp = (const GAS v4u*)(w_gate + (size_t)(64 * rr) * FF + h * FS + 8 * cc);
      const GAS v4u* up = (const GAS v4u*)(w_up + (size_t)(64 * rr) * FF + h * FS + 8 * cc);
      float ag[8], au[8];
#pragma unroll
      for (int j = 0; j < 8; ++j) { ag[j] = 0.f; au[j] = 0.f; }
#pragma unroll 1
      for (int it = 0; it < 16; ++it) {
        v4u Gc[4], Uc[4];
#pragma unroll
        for (int t = 0; t < 4; ++t) { Gc[t] = gp[(it * 4 + t) * (FF / 8)]; Uc[t] = up[(it * 4 + t) * (FF / 8)]; }
        const f32x4 xq = *(const LAS f32x4*)(xs + 64 * rr + 4 * it);
#pragma unroll
        for (int t = 0; t < 4; ++t) {
          const float xt = xq[t];
#pragma unroll
          for (int j = 0; j < 4; ++j) {
            ag[2 * j] = fmaf(xt, bflo(Gc[t][j]), ag[2 * j]); ag[2 * j + 1] = fmaf(xt, bfhi(Gc[t][j]), ag[2 * j + 1]);
            au[2 * j] = fmaf(xt, bflo(Uc[t][j]), au[2 * j]); au[2 * j + 1] = fmaf(xt, bfhi(Uc[t][j]), au[2 * j + 1]);
          }
        }
        __builtin_amdgcn_s_sleep(MOE_SLEEP);
      }
      LAS float* act = (LAS float*)(L + L_ACT);
#pragma unroll
      for (int j = 0; j < 8; ++j) {
        float gg = ag[j], uu = au[j];
        gg += dpp<XOR8>(gg); uu += dpp<XOR8>(uu);
        gg = rbf(xrow_sum(gg)); uu = rbf(xrow_sum(uu));
        const float si = rbf(gg * sigmoidf_(gg));
        if (lane < 8) act[8 * lane + j] = rbf(si * uu);
      }
      const GAS v4u* dp = (const GAS v4u*)(w_down + (size_t)(h * FS) * DM + lane * 8);
#pragma unroll 1
      for (int it = 0; it < 8; ++it) {
        v4u Dc[8];
#pragma unroll
        for (int t = 0; t < 8; ++t) Dc[t] = dp[(it * 8 + t) * (DM / 8)];
#pragma unroll
        for (int t = 0; t < 8; ++t) {
          const float a = act[it * 8 + t];
#pragma unroll
          for (int j = 0; j < 4; ++j) { y[2 * j] = fmaf(a, bflo(Dc[t][j]), y[2 * j]); y[2 * j + 1] = fmaf(a, bfhi(Dc[t][j]), y[2 * j + 1]); }
        }
        __builtin_amdgcn_s_sleep(MOE_SLEEP);
      }
    }
    const float f = omg * rs;
    LAS f32x4* ym = (LAS f32x4*)(L + L_YMOE) + 2 * lane;
    ym[0] = f32x4{y[0] * f, y[1] * f, y[2] * f, y[3] * f}; ym[1] = f32x4{y[4] * f, y[5] * f, y[6] * f, y[7] * f};
    {
      GAS u64* rdy = (GAS u64*)(ws + WS_READY) + b * NH + (lane & 31);
      for (unsigned spins = 0; spins < SPIN_CAP; ++spins) {
        const u64 v = __hip_atomic_load(rdy, RLX_AGENT);
        if (__all(v == tag64)) break;
        __builtin_amdgcn_s_sleep(8);
      }
    }
    wait_lgkm();
    if (lane == 0) ctl[4] = 1u;
    raw_barrier();
  }
  {
    const u64 tag64 = ((const LAS u64*)(L + L_SCAL))[1];
    const unsigned tag32 = tag32_of(tag64);
    const float omg = ((const LAS float*)(L + L_SCAL))[1];
    GAS u64* src = (GAS u64*)(ws + WS_INBOX) + ((size_t)(b * NH + h) * NH + 8 * r) * 16 + c;
    const float xres = bflo((unsigned)xg[b * DM + 16 * h + c]);
    if (w == 0) __builtin_amdgcn_s_sleep(POLL_DELAY); else if (w == NCONS + 1) __builtin_amdgcn_s_sleep(POLL_DELAY + POLL_STAG); else __builtin_amdgcn_s_sleep(POLL_DELAY + 2 * POLL_STAG);
    for (unsigned spins = 0; spins < SPIN_CAP; ++spins) {
      bool ok = true;
      float s = 0.f;
#pragma unroll
      for (int i = 0; i < 8; ++i) { const u64 x = __hip_atomic_load(src + i * 16, RLX_AGENT); s += u2f((unsigned)x); ok &= (unsigned)(x >> 32) == tag32; }
      s = xrow_sum(s);
      s = fmaf(omg, xres, s);
      unsigned o2 = pack_bf16(s, dpp<XOR1>(s));
      asm volatile("" : "+v"(o2));
      if (__all(ok)) {
        if (lane < 16 && (lane & 1) == 0) __hip_atomic_store((GAS unsigned*)(out + b * DM + 16 * h + lane), o2, RLX_AGENT);
        break;
      }
    }
    if (LEAK != 0 && w == 0 && bid == LEAK_BID) {
      const u64 now = __builtin_amdgcn_s_memrealtime();
      if (LEAK == 1) lk_x = now;
      if (LEAK == 3) lk_x = lk_t0 + __builtin_amdgcn_readfirstlane(ctl[7]);
      long long extra = 10ll * ((long long)(lk_x - lk_t0) - (long long)LEAK_OFF);
      if (extra < 0) extra = 0;
      if (extra > 40000) extra = 40000;
      while ((long long)(__builtin_amdgcn_s_memrealtime() - now) < extra) __builtin_amdgcn_s_sleep(1);
    }
    if (w == 0 && bid == 0 && lane == 0) __hip_atomic_store((GAS u64*)(ws + WS_EPOCH), tag64, RLX_AGENT);
  }
}
}

extern "C" void kernel_launch(void* const* d_in, const int* in_sizes, int n_in, void* d_out, int out_size, void* d_ws, size_t ws_size, hipStream_t stream) {
  (void)in_sizes; (void)n_in; (void)out_size; (void)ws_size;
  (void)hipFuncSetAttribute((const void*)fused_kernel, hipFuncAttributeMaxDynamicSharedMemorySize, LDS_BYTES);
  fused_kernel<<<dim3(NB * NH), dim3(NTHR), LDS_BYTES, stream>>>(
      (const u16*)d_in[0], (const u16*)d_in[1], (const u16*)d_in[2], (const u16*)d_in[3], (const u16*)d_in[4],
      (const u16*)d_in[6], (const u16*)d_in[7], (const u16*)d_in[8], (const u16*)d_in[9], (const u16*)d_in[10], (u16*)d_out, (unsigned char*)d_ws);
}
```

```cpp
#include <hip/hip_runtime.h>
#include <stdint.h>
#include <type_traits>

#define GAS __attribute__((address_space(1)))
#define LAS __attribute__((address_space(3)))
typedef unsigned short u16;
typedef unsigned long long u64;
typedef unsigned v4u __attribute__((ext_vector_type(4)));
typedef unsigned v2u __attribute__((ext_vector_type(2)));
typedef float f32x4 __attribute__((ext_vector_type(4)));
typedef float f32x2 __attribute__((ext_vector_type(2)));
typedef __bf16 bf16x2 __attribute__((ext_vector_type(2)));
typedef __bf16 bf16x8 __attribute__((ext_vector_type(8)));

namespace {
constexpr int NB = 8, NH = 32, SK = 4096, DH = 128, DM = 512, FF = 1792, FS = 64;
constexpr int NCONS = 8, NWAVES = NCONS + 3, NTHR = 64 * NWAVES;
constexpr float SM_C = 0.088388347648318440f * 1.4426950408889634f;
constexpr int NPIECE = 2048;
constexpr int RING = 128;
constexpr unsigned SPIN_CAP = 1u << 21;
constexpr int INFL = 24, W_M2 = 22, W_M1 = 24, W_P1 = 32, W_P2 = 44;
constexpr int FIRST = 24;
constexpr int PB = 2;
constexpr int BLK = 32;
constexpr int TH1 = 10, TH2 = 28, TH1L = 6, TH2L = 16, GOV_LATE = 1500, GOV_EXIT = 128, GOV_SLEEP = 100, GOV_SLEEP2 = 30, NPEER = 8;
constexpr unsigned long long GOV_RATE = 283ull;
constexpr float GOV_ALPHA = 0.5f;
constexpr int Q_DELAY = 4, Q_POLL = 2, POLL_DELAY = 6, POLL_STAG = 4, MOE_SLEEP = 20;
static_assert(FIRST % 4 == 0 && BLK % 4 == 0 && 1024 % BLK == 0, "the loader works in groups of four pieces that never straddle the ring's end");

constexpr int L_RING = 0;
constexpr int L_SC = RING * 1024;
constexpr int L_QPART = L_SC + 8 * 512 * 4;
constexpr int L_ML = L_QPART + 8 * 128 * 4;
constexpr int L_CTL = L_ML + 64;
constexpr int L_SCAL = L_CTL + 64;
constexpr int L_ACT = L_SCAL + 64;
constexpr int L_YMOE = L_ACT + 256;
constexpr int L_XS = L_YMOE + 512 * 4;
constexpr int LDS_BYTES = L_XS + 512 * 4;
static_assert(LDS_BYTES <= 160 * 1024, "lds");

constexpr size_t WS_EPOCH = 0;
constexpr size_t WS_READY = 4096;
constexpr size_t WS_PROG = 32768;
constexpr size_t WS_INBOX = 65536;
constexpr size_t WS_QIN = (size_t)2 << 20;

#define RLX_AGENT __ATOMIC_RELAXED, __HIP_MEMORY_SCOPE_AGENT

__device__ __forceinline__ float u2f(unsigned u) { return __builtin_bit_cast(float, u); }
__device__ __forceinline__ unsigned f2u(float f) { return __builtin_bit_cast(unsigned, f); }
__device__ __forceinline__ float bflo(unsigned u) { return u2f(u << 16); }
__device__ __forceinline__ float bfhi(unsigned u) { return u2f(u & 0xffff0000u); }
__device__ __forceinline__ float dot2(unsigned a, unsigned b, float acc) {
  return __builtin_amdgcn_fdot2_f32_bf16(__builtin_bit_cast(bf16x2, a), __builtin_bit_cast(bf16x2, b), acc, false);
}
__device__ __forceinline__ f32x4 mfma16(v4u a, v4u b, f32x4 c) {
  return __builtin_amdgcn_mfma_f32_16x16x32_bf16(__builtin_bit_cast(bf16x8, a), __builtin_bit_cast(bf16x8, b), c, 0, 0, 0);
}
__device__ __forceinline__ unsigned pack_bf16(float lo, float hi) {
  bf16x2 v = {(__bf16)lo, (__bf16)hi};
  return __builtin_bit_cast(unsigned, v);
}
__device__ __forceinline__ float rbf(float x) { return u2f((unsigned)__builtin_bit_cast(u16, (__bf16)x) << 16); }
template <int CTRL> __device__ __forceinline__ unsigned dppu(unsigned x) { return (unsigned)__builtin_amdgcn_mov_dpp((int)x, CTRL, 0xf, 0xf, true); }
template <int CTRL> __device__ __forceinline__ float dpp(float x) { return u2f(dppu<CTRL>(f2u(x))); }
constexpr int XOR1 = 0xB1, XOR2 = 0x4E, XOR7 = 0x141, XOR8 = 0x128, BCAST16 = 0x150;
__device__ __forceinline__ float xrow_sum(float x) {
  auto s = __builtin_amdgcn_permlane16_swap(f2u(x), f2u(x), false, false);
  x = u2f(s[0]) + u2f(s[1]);
  auto t = __builtin_amdgcn_permlane32_swap(f2u(x), f2u(x), false, false);
  return u2f(t[0]) + u2f(t[1]);
}
__device__ __forceinline__ float xrow_max(float x) {
  auto s = __builtin_amdgcn_permlane16_swap(f2u(x), f2u(x), false, false);
  x = fmaxf(u2f(s[0]), u2f(s[1]));
  auto t = __builtin_amdgcn_permlane32_swap(f2u(x), f2u(x), false, false);
  return fmaxf(u2f(t[0]), u2f(t[1]));
}
__device__ __forceinline__ float row_sum(float x) { x += dpp<XOR8>(x); x += dpp<XOR7>(x); x += dpp<XOR2>(x); x += dpp<XOR1>(x); return x; }
__device__ __forceinline__ float row_max(float x) { x = fmaxf(x, dpp<XOR8>(x)); x = fmaxf(x, dpp<XOR7>(x)); x = fmaxf(x, dpp<XOR2>(x)); x = fmaxf(x, dpp<XOR1>(x)); return x; }
__device__ __forceinline__ float wave_sum(float x) { return xrow_sum(row_sum(x)); }
__device__ __forceinline__ float wave_max(float x) { return xrow_max(row_max(x)); }
__device__ __forceinline__ float dot8(const v4u k, const v4u q) {
  float d = dot2(k.x, q.x, 0.f); d = dot2(k.y, q.y, d); d = dot2(k.z, q.z, d); d = dot2(k.w, q.w, d); return d;
}
__device__ __forceinline__ float treduce16(float (&x)[16], int c) {
#define TR_STEP(HALF, CTRL, BIT) _Pragma("unroll") for (int h = 0; h < (HALF); ++h) { \
    const float lo_ = x[h], hi_ = x[h + (HALF)]; const float t1_ = lo_ + dpp<CTRL>(lo_), t2_ = hi_ + dpp<CTRL>(hi_); x[h] = (c & (BIT)) ? t2_ : t1_; }
  TR_STEP(8, XOR8, 8) TR_STEP(4, XOR7, 4) TR_STEP(2, XOR2, 2) TR_STEP(1, XOR1, 1)
#undef TR_STEP
  return x[0];
}
__device__ __forceinline__ float sigmoidf_(float v) { return 1.0f / (1.0f + __expf(-v)); }
template <int T> __device__ __forceinline__ void pv_step(f32x2 (&acc)[4], const v4u v, float p) {
  const float pb = dpp<BCAST16 + T>(p);
  const f32x2 p2 = {pb, pb};
#pragma unroll
  for (int j = 0; j < 4; ++j) acc[j] = __builtin_elementwise_fma(p2, f32x2{bflo(v[j]), bfhi(v[j])}, acc[j]);
}
template <int T, int N> struct Unroll { template <class F> static __device__ __forceinline__ void run(F&& f) { f(std::integral_constant<int, T>{}); Unroll<T + 1, N>::run(f); } };
template <int N> struct Unroll<N, N> { template <class F> static __device__ __forceinline__ void run(F&&) {} };
template <int N> __device__ __forceinline__ void wait_vm() { asm volatile("s_waitcnt vmcnt(%0)" ::"n"(N) : "memory"); }
__device__ __forceinline__ void wait_lgkm() { asm volatile("s_waitcnt lgkmcnt(0)" ::: "memory"); }
__device__ __forceinline__ void raw_barrier() { asm volatile("s_waitcnt lgkmcnt(0)\n\ts_barrier" ::: "memory"); }
template <int SLEEP> __device__ __forceinline__ void wait_ge(volatile LAS unsigned* word, unsigned need) {
  for (unsigned spins = 0; spins < SPIN_CAP; ++spins) {
    if ((unsigned)__builtin_amdgcn_readfirstlane(*word) >= need) break;
    if (SLEEP) __builtin_amdgcn_s_sleep(SLEEP);
  }
  asm volatile("" ::: "memory");
}
__device__ __forceinline__ void wait_landed(volatile LAS unsigned* ctl, unsigned need, unsigned& have) {
  if (have >= need) return;
  for (unsigned spins = 0; spins < SPIN_CAP; ++spins) {
    have = __builtin_amdgcn_readfirstlane(ctl[0]);
    if (have >= need) break;
    __builtin_amdgcn_s_sleep(1);
  }
  asm volatile("" ::: "memory");
}
__device__ __forceinline__ int min_consumed(volatile LAS unsigned* ctl, int lane) {
  unsigned cn = ctl[8 + (lane & 7)];
  cn = min(cn, dppu<XOR1>(cn)); cn = min(cn, dppu<XOR2>(cn)); cn = min(cn, dppu<XOR7>(cn));
  return (int)__builtin_amdgcn_readfirstlane(cn);
}
__device__ __forceinline__ unsigned tag32_of(u64 tag64) { return (unsigned)tag64 ^ (unsigned)(tag64 >> 32); }

__global__ void __launch_bounds__(NTHR, 3) fused_kernel(const u16* __restrict__ xg, const u16* __restrict__ Kg, const u16* __restrict__ Vg, const u16* __restrict__ wq,
                                                     const u16* __restrict__ wo, const u16* __restrict__ w_r, const u16* __restrict__ w_gate, const u16* __restrict__ w_up,
                                                     const u16* __restrict__ w_down, const u16* __restrict__ gate_w, u16* __restrict__ out, unsigned char* __restrict__ ws) {
  extern __shared__ __attribute__((aligned(16))) unsigned char smem[];
  LAS unsigned char* L = (LAS unsigned char*)smem;
  const int tid = threadIdx.x, lane = tid & 63, w = __builtin_amdgcn_readfirstlane(tid >> 6);
  const int bid = blockIdx.x;
  const int h = (bid & 7) * 4 + (bid >> 6), b = (bid >> 3) & 7;
  const int r = lane >> 4, c = lane & 15;
  volatile LAS unsigned* ctl = (volatile LAS unsigned*)(L + L_CTL);
  if (tid < 16) ctl[tid] = 0u;
  asm volatile("" ::"s"(Kg), "s"(Vg), "s"(wq), "s"(xg), "s"(ws));
  raw_barrier();

  if (__builtin_expect(w == 0, 1)) {
    __builtin_amdgcn_s_setprio(3);
    const size_t kvoff = (size_t)(b * NH + h) * SK * DH;
    const __amdgpu_buffer_rsrc_t rsK = __builtin_amdgcn_make_buffer_rsrc((void*)(Kg + kvoff), 0, SK * DH * 2, 0x00020000);
    const __amdgpu_buffer_rsrc_t rsV = __builtin_amdgcn_make_buffer_rsrc((void*)(Vg + kvoff), 0, SK * DH * 2, 0x00020000);
    const int voff = lane * 16;
#pragma unroll
    for (int i = 0; i < FIRST; ++i) __builtin_amdgcn_raw_ptr_buffer_load_lds(rsK, (LAS void*)(L + L_RING + i * 1024), 16, voff, i * 1024, 0, 2);
    int free_until = RING, pub = 0;
    auto make_room = [&](int gp) {
      if (gp + 4 > free_until) {
        free_until = 8 * min_consumed(ctl, lane) + RING;
        if (gp + 4 > free_until) {
          wait_vm<0>();
          pub = gp;
          ctl[0] = (unsigned)gp;
          for (unsigned spins = 0; spins < SPIN_CAP; ++spins) {
            free_until = 8 * min_consumed(ctl, lane) + RING;
            if (gp + 4 <= free_until) break;
            __builtin_amdgcn_s_sleep(1);
          }
        }
        asm volatile("" ::: "memory");
      }
    };
    auto run_block = [&](auto WW, const __amdgpu_buffer_rsrc_t rs, int goff, int p0, int p1) {
      constexpr int W = decltype(WW)::value;
#pragma unroll 1
      for (int p = p0; p < p1; p += 4) {
        const int gp = goff + p;
        make_room(gp);
        LAS unsigned char* dst = L + L_RING + (gp & (RING - 1)) * 1024;
#pragma unroll
        for (int i = 0; i < 4; ++i) __builtin_amdgcn_raw_ptr_buffer_load_lds(rs, (LAS void*)(dst + i * 1024), 16, voff, (p + i) * 1024, 0, 2);
        wait_vm<W>();
        pub = max(pub, gp + 4 - W);
        ctl[0] = (unsigned)pub;
      }
    };
    run_block(std::integral_constant<int, INFL>{}, rsK, 0, FIRST, ((FIRST + BLK - 1) / BLK) * BLK);
    for (int half = 0; half < 2; ++half) {
      const __amdgpu_buffer_rsrc_t rs = half ? rsV : rsK;
#pragma unroll 1
      for (int p = half ? 0 : ((FIRST + BLK - 1) / BLK) * BLK; p < 1024; p += BLK) {
        const int cl = __builtin_amdgcn_readfirstlane(ctl[2]);
        if (cl == 0) run_block(std::integral_constant<int, INFL>{}, rs, half * 1024, p, p + BLK);
        else if (cl == 1) run_block(std::integral_constant<int, W_M2>{}, rs, half * 1024, p, p + BLK);
        else if (cl == 2) run_block(std::integral_constant<int, W_M1>{}, rs, half * 1024, p, p + BLK);
        else if (cl == 3) run_block(std::integral_constant<int, W_P1>{}, rs, half * 1024, p, p + BLK);
        else run_block(std::integral_constant<int, W_P2>{}, rs, half * 1024, p, p + BLK);
      }
    }
    Unroll<0, 10>::run([&](auto I) {
      constexpr int i = decltype(I)::value;
      constexpr int n = i == 0 ? 32 : i == 1 ? 24 : i == 2 ? 18 : i == 3 ? 14 : i == 4 ? 10 : i == 5 ? 7 : i == 6 ? 4 : i == 7 ? 2 : i == 8 ? 1 : 0;
      wait_vm<n>();
      pub = max(pub, NPIECE - n);
      ctl[0] = (unsigned)pub;
    });
    raw_barrier();
  } else if (w <= NCONS) {
    const int cw = w - 1;
    const LAS v4u* rp = (const LAS v4u*)(L + L_RING + cw * 1024) + lane;
    unsigned have = 0;
    v4u P[4 * PB];
#pragma unroll
    for (int sub = 0; sub < PB; ++sub) {
      wait_landed(ctl, (unsigned)(8 * (4 * sub + 3) + cw + 1), have);
#pragma unroll
      for (int i = 0; i < 4; ++i) P[4 * sub + i] = rp[(4 * sub + i) * 512];
      wait_lgkm();
      asm volatile("" : "+v"(P[4 * sub]), "+v"(P[4 * sub + 1]), "+v"(P[4 * sub + 2]), "+v"(P[4 * sub + 3]));
      ctl[8 + cw] = (unsigned)(4 * sub + 4);
    }
    v4u q;
    {
      wait_ge<1>(ctl + 6, 1u);
      __builtin_amdgcn_s_sleep(Q_DELAY);
      const u64 tag40 = ((const LAS u64*)(L + L_SCAL))[1] & 0xFFFFFFFFFFull;
      GAS u64* src = (GAS u64*)(ws + WS_QIN) + (size_t)((h * NB + b) * 8 + cw) * 128 + lane;
      u64 g0 = 0, g1 = 0;
      for (unsigned spins = 0; spins < SPIN_CAP; ++spins) {
        g0 = __hip_atomic_load(src, RLX_AGENT); g1 = __hip_atomic_load(src + 64, RLX_AGENT);
        if (__all((g0 >> 24) == tag40 && (g1 >> 24) == tag40)) break;
        __builtin_amdgcn_s_sleep(Q_POLL);
      }
      LAS float* qp = (LAS float*)(L + L_QPART) + cw * 128 + 8 * c + r;
      qp[0] = u2f((unsigned)g0 << 8); qp[4] = u2f((unsigned)g1 << 8);
      wait_lgkm();
      if (lane == 0) __hip_atomic_fetch_add((LAS unsigned*)(L + L_CTL) + 1, 1u, __ATOMIC_RELAXED, __HIP_MEMORY_SCOPE_WORKGROUP);
      wait_ge<0>(ctl + 1, (unsigned)NCONS);
      f32x4 s0 = {0.f, 0.f, 0.f, 0.f}, s1 = {0.f, 0.f, 0.f, 0.f};
#pragma unroll
      for (int ww = 0; ww < 8; ++ww) { const LAS f32x4* p4 = (const LAS f32x4*)(L + L_QPART) + ww * 32 + c * 2; s0 += p4[0]; s1 += p4[1]; }
      q = v4u{pack_bf16(s0[0], s0[1]), pack_bf16(s0[2], s0[3]), pack_bf16(s1[0], s1[1]), pack_bf16(s1[2], s1[3])};
    }
    LAS float* sc = (LAS float*)(L + L_SC) + cw * 512 + lane;
    float mx = -INFINITY;
    auto kgroup = [&](int g) {
      float x[16];
#pragma unroll
      for (int sub = 0; sub < 4; ++sub) {
        wait_landed(ctl, (unsigned)(8 * (16 * g + 4 * sub + 3) + cw + 1), have);
        v4u d[4];
#pragma unroll
        for (int i = 0; i < 4; ++i) d[i] = rp[(4 * sub + i) * 512];
        wait_lgkm();
        asm volatile("" : "+v"(d[0]), "+v"(d[1]), "+v"(d[2]), "+v"(d[3]));
        ctl[8 + cw] = (unsigned)(16 * g + 4 * sub + 4);
#pragma unroll
        for (int i = 0; i < 4; ++i) x[4 * sub + i] = dot8(d[i], q);
      }
      const float s = treduce16(x, c); sc[g * 64] = s; mx = fmaxf(mx, s);
    };
    const GAS v4u* wop = (const GAS v4u*)(wo + (size_t)(h * DH + 32 * r + (c & 1)) * DM + 64 * cw + 8 * (c >> 1));
    {
      float x[16];
#pragma unroll
      for (int t = 0; t < 4 * PB; ++t) x[t] = dot8(P[t], q);
#pragma unroll
      for (int sub = PB; sub < 4; ++sub) {
        wait_landed(ctl, (unsigned)(8 * (4 * sub + 3) + cw + 1), have);
        v4u d[4];
#pragma unroll
        for (int i = 0; i < 4; ++i) d[i] = rp[(4 * sub + i) * 512];
        wait_lgkm();
        asm volatile("" : "+v"(d[0]), "+v"(d[1]), "+v"(d[2]), "+v"(d[3]));
        ctl[8 + cw] = (unsigned)(4 * sub + 4);
#pragma unroll
        for (int i = 0; i < 4; ++i) x[4 * sub + i] = dot8(d[i], q);
      }
      const float s = treduce16(x, c); sc[0] = s; mx = s;
    }
    v4u Wo[16];
    const int g1 = 1 + (h % 3), g2 = 4 + (h % 3) + ((h & 4) >> 2);
#pragma unroll 1
    for (int g = 1; g < g1; ++g) kgroup(g);
#pragma unroll
    for (int t = 0; t < 8; ++t) Wo[t] = wop[t * (2 * DM / 8)];
    __builtin_amdgcn_sched_barrier(0);
#pragma unroll 1
    for (int g = g1; g < g2; ++g) kgroup(g);
#pragma unroll
    for (int t = 8; t < 16; ++t) Wo[t] = wop[t * (2 * DM / 8)];
    __builtin_amdgcn_sched_barrier(0);
#pragma unroll 1
    for (int g = g2; g < 8; ++g) kgroup(g);
    LAS float* ml = (LAS float*)(L + L_ML);
    {
      const float m = wave_max(mx);
      if (lane == 0) ml[cw] = m;
      wait_lgkm();
      if (lane == 0) __hip_atomic_fetch_add((LAS unsigned*)(L + L_CTL) + 3, 1u, __ATOMIC_RELAXED, __HIP_MEMORY_SCOPE_WORKGROUP);
    }
    unsigned Wp[16][4];
    {
      const bool odd = (c & 1) != 0;
      const unsigned sel_lo = odd ? 0x01000504u : 0x05040100u, sel_hi = odd ? 0x03020706u : 0x07060302u;
#pragma unroll
      for (int i = 0; i < 16; ++i)
#pragma unroll
        for (int j = 0; j < 2; ++j) {
          const unsigned keep = odd ? Wo[i][2 + j] : Wo[i][j], send = odd ? Wo[i][j] : Wo[i][2 + j];
          const unsigned recv = dppu<XOR1>(send);
          Wp[i][2 * j] = __builtin_amdgcn_perm(recv, keep, sel_lo);
          Wp[i][2 * j + 1] = __builtin_amdgcn_perm(recv, keep, sel_hi);
          asm volatile("" : "+v"(Wp[i][2 * j]), "+v"(Wp[i][2 * j + 1]));
        }
    }
    wait_ge<0>(ctl + 3, (unsigned)NCONS);
    {
      const f32x4 m0 = *(const LAS f32x4*)ml, m1 = *(const LAS f32x4*)(ml + 4);
      const float M = fmaxf(fmaxf(fmaxf(m0[0], m0[1]), fmaxf(m0[2], m0[3])), fmaxf(fmaxf(m1[0], m1[1]), fmaxf(m1[2], m1[3])));
      const float mc = M * SM_C;
      float lsum = 0.f;
#pragma unroll
      for (int g = 0; g < 8; ++g) { const float p = __builtin_amdgcn_exp2f(fmaf(sc[g * 64], SM_C, -mc)); sc[g * 64] = p; lsum += p; }
      lsum = wave_sum(lsum);
      if (lane == 0) ml[8 + cw] = lsum;
    }
    f32x2 acc2[4];
#pragma unroll
    for (int j = 0; j < 4; ++j) acc2[j] = f32x2{0.f, 0.f};
    auto vbatch = [&](auto S, int g, float p) {
      constexpr int sub = decltype(S)::value;
      wait_landed(ctl, (unsigned)(1024 + 8 * (16 * g + 4 * sub + 3) + cw + 1), have);
      v4u d[4];
#pragma unroll
      for (int i = 0; i < 4; ++i) d[i] = rp[(4 * sub + i) * 512];
      wait_lgkm();
      asm volatile("" : "+v"(d[0]), "+v"(d[1]), "+v"(d[2]), "+v"(d[3]));
      ctl[8 + cw] = (unsigned)(128 + 16 * g + 4 * sub + 4);
      pv_step<4 * sub + 0>(acc2, d[0], p); pv_step<4 * sub + 1>(acc2, d[1], p); pv_step<4 * sub + 2>(acc2, d[2], p); pv_step<4 * sub + 3>(acc2, d[3], p);
      asm volatile("" : "+v"(acc2[0]), "+v"(acc2[1]), "+v"(acc2[2]), "+v"(acc2[3]));
    };
    float plast = 0.f;
#pragma unroll 1
    for (int g = 0; g < 8; ++g) {
      const float p = sc[g * 64];
      plast = p;
      vbatch(std::integral_constant<int, 0>{}, g, p); vbatch(std::integral_constant<int, 1>{}, g, p); vbatch(std::integral_constant<int, 2>{}, g, p);
      if (g < 7) vbatch(std::integral_constant<int, 3>{}, g, p);
    }
    wait_ge<1>(ctl + 4, 1u);
    const int col = 64 * cw + 4 * c + r;
    const float ymoe = ((const LAS float*)(L + L_YMOE))[col];
    const float gate = ((const LAS float*)(L + L_SCAL))[0];
    const unsigned tag32 = tag32_of(((const LAS u64*)(L + L_SCAL))[1]);
    GAS u64* dst = (GAS u64*)(ws + WS_INBOX) + ((size_t)(b * NH + (col >> 4)) * NH + h) * 16 + (col & 15);
    const f32x4 ls = *(const LAS f32x4*)(ml + 8), lt = *(const LAS f32x4*)(ml + 12);
    const float inv = 1.0f / (((ls[0] + ls[1]) + (ls[2] + ls[3])) + ((lt[0] + lt[1]) + (lt[2] + lt[3])));
    Unroll<12, 16>::run([&](auto T) {
      constexpr int t = decltype(T)::value;
      wait_landed(ctl, (unsigned)(1024 + 8 * (16 * 7 + t) + cw + 1), have);
      const v4u d = rp[t * 512];
      pv_step<t>(acc2, d, plast);
    });
    {
      float u[4];
#pragma unroll
      for (int j = 0; j < 2; ++j)
#pragma unroll
        for (int e = 0; e < 2; ++e) {
          auto t = __builtin_amdgcn_permlane32_swap(f2u(acc2[j][e]), f2u(acc2[j + 2][e]), false, false);
          u[2 * j + e] = u2f(t[0]) + u2f(t[1]);
        }
      f32x2 o2;
#pragma unroll
      for (int e = 0; e < 2; ++e) {
        auto t = __builtin_amdgcn_permlane16_swap(f2u(u[e]), f2u(u[2 + e]), false, false);
        o2[e] = u2f(t[0]) + u2f(t[1]);
      }
      *(LAS f32x2*)(L + L_QPART + (cw * 128 + 8 * c + 2 * r) * 4) = o2;
    }
    raw_barrier();
    {
      f32x2 o2 = {0.f, 0.f};
#pragma unroll
      for (int ww = 0; ww < 8; ++ww) o2 += *(const LAS f32x2*)(L + L_QPART + (ww * 128 + 2 * lane) * 4);
      const unsigned op = pack_bf16(o2[0] * inv, o2[1] * inv);
      f32x4 D[4];
#pragma unroll
      for (int j = 0; j < 4; ++j) D[j] = f32x4{0.f, 0.f, 0.f, 0.f};
      Unroll<0, 4>::run([&](auto S) {
        constexpr int s = decltype(S)::value;
        const v4u A = {dppu<BCAST16 + 4 * s>(op), dppu<BCAST16 + 4 * s + 1>(op), dppu<BCAST16 + 4 * s + 2>(op), dppu<BCAST16 + 4 * s + 3>(op)};
#pragma unroll
        for (int j = 0; j < 4; ++j) D[j] = mfma16(A, v4u{Wp[4 * s][j], Wp[4 * s + 1][j], Wp[4 * s + 2][j], Wp[4 * s + 3][j]}, D[j]);
      });
      const float d0 = D[0][0], d1 = D[1][0], d2 = D[2][0], d3 = D[3][0];
      const float ycol = (r & 2) ? ((r & 1) ? d3 : d2) : ((r & 1) ? d1 : d0);
      __hip_atomic_store(dst, ((u64)tag32 << 32) | f2u(fmaf(ycol, gate, ymoe)), RLX_AGENT);
    }
    return;
  } else if (w == NCONS + 2) {
    wait_ge<2>(ctl + 5, 1u);
    const unsigned tagb = (unsigned)__builtin_amdgcn_readfirstlane((unsigned)((const LAS u64*)(L + L_SCAL))[1]) & 0xffffu;
    GAS unsigned* board = (GAS unsigned*)(ws + WS_PROG);
    float gavg = 0.f;
    unsigned me_prev = 0, last = 0; bool have_avg = false;
    for (unsigned it = 0; it < 8192; ++it) {
      if (last < (unsigned)GOV_LATE) __builtin_amdgcn_s_sleep(GOV_SLEEP); else __builtin_amdgcn_s_sleep(GOV_SLEEP2);
      const unsigned my = __builtin_amdgcn_readfirstlane(ctl[0]);
      last = my;
      if (my >= (unsigned)(NPIECE - GOV_EXIT)) break;
      const unsigned me = (my - (unsigned)((__builtin_amdgcn_s_memrealtime() * GOV_RATE) >> 10)) & 0xffffu;
      if (lane == 0) __hip_atomic_store(board + bid * 32, (tagb << 16) | me, RLX_AGENT);
      unsigned ev[NPEER];
      {
        const unsigned char* bb = (const unsigned char*)(ws + WS_PROG);
#pragma unroll
        for (int j = 0; j < NPEER; ++j) {
          const unsigned off = (unsigned)__builtin_amdgcn_readfirstlane(((bid + 1 + 33 * j + 7 * (int)it) & 255) * 128);
          asm volatile("s_load_dword %0, %1, %2 glc" : "=&s"(ev[j]) : "s"(bb), "s"(off) : "memory");
        }
        asm volatile("s_waitcnt lgkmcnt(0)" ::: "memory");
#pragma unroll
        for (int j = 0; j < NPEER; ++j) asm volatile("" : "+s"(ev[j]));
      }
      float sum = 0.f, cnt = 0.f;
#pragma unroll
      for (int j = 0; j < NPEER; ++j) { const bool ok = (ev[j] >> 16) == tagb; sum += ok ? (float)(short)(u16)(ev[j] - me) : 0.f; cnt += ok ? 1.f : 0.f; }
      const float smp = sum / fmaxf(cnt, 1.f);
      if (cnt >= (float)(NPEER / 2)) {
        const float prev = have_avg ? gavg - (float)(short)(u16)(me - me_prev) : smp;
        gavg = prev + (float)GOV_ALPHA * (smp - prev);
        me_prev = me; have_avg = true;
        const float diff = -gavg;
        const bool late = my >= (unsigned)GOV_LATE;
        const float t1 = late ? (float)TH1L : (float)TH1, t2 = late ? (float)TH2L : (float)TH2;
        const unsigned cls = (diff >= t2) ? 1u : (diff >= t1) ? 2u : (diff <= -t2) ? 4u : (diff <= -t1) ? 3u : 0u;
        if (lane == 0) ctl[2] = cls;
      }
    }
    raw_barrier();
  } else {
    const u64 e0 = __hip_atomic_load((GAS u64*)(ws + WS_EPOCH), RLX_AGENT);
    const GAS v4u* xap = (const GAS v4u*)(xg + (lane & 7) * DM + 64 * b + 8 * r);
    const v4u xa0 = xap[0], xa1 = xap[4];
    const GAS v4u* wqp = (const GAS v4u*)(wq + (size_t)(64 * b + 8 * r) * (NH * DH) + h * DH + 8 * c);
    v4u W[16];
#pragma unroll
    for (int t = 0; t < 16; ++t) W[t] = wqp[(size_t)((t >> 3) * 32 + (t & 7)) * (NH * DH / 8)];
    __builtin_amdgcn_sched_barrier(0);
    const u64 tag64 = e0 + 1ull;
    if (lane == 0) { ((LAS u64*)(L + L_SCAL))[1] = tag64; wait_lgkm(); ctl[5] = 1u; }
    {
      f32x4 D[8];
#pragma unroll
      for (int j = 0; j < 8; ++j) D[j] = f32x4{0.f, 0.f, 0.f, 0.f};
#pragma unroll
      for (int u = 0; u < 2; ++u)
#pragma unroll
        for (int j = 0; j < 8; ++j) {
          const unsigned sel = (j & 1) ? 0x07060302u : 0x05040100u;
          v4u Bf;
#pragma unroll
          for (int qq = 0; qq < 4; ++qq) Bf[qq] = __builtin_amdgcn_perm(W[8 * u + 2 * qq + 1][j >> 1], W[8 * u + 2 * qq][j >> 1], sel);
          D[j] = mfma16(u ? xa1 : xa0, Bf, D[j]);
        }
      const u64 tg = (tag64 & 0xFFFFFFFFFFull) << 24;
      GAS u64* qdst = (GAS u64*)(ws + WS_QIN) + (size_t)((h * NB + 4 * (r & 1)) * 8 + b) * 128 + (r >> 1) * 64 + c;
#pragma unroll
      for (int i = 0; i < 4; ++i)
#pragma unroll
        for (int jj = 0; jj < 4; ++jj) {
          const float lo = D[jj][i], hi = D[jj + 4][i];
          __hip_atomic_store(qdst + (size_t)i * 8 * 128 + jj * 16, tg | (u64)(f2u((r & 2) ? hi : lo) >> 8), RLX_AGENT);
        }
    }
    if (lane == 0) ctl[6] = 1u;
    const GAS v4u* xp = (const GAS v4u*)(xg + b * DM + 8 * lane);
    const v4u xv = xp[0];
    const v4u gw = *(const GAS v4u*)(gate_w + 8 * lane);
    const v4u wr = *(const GAS v4u*)(w_r + 8 * lane);
    const unsigned tag32 = tag32_of(tag64);
    {
      const __amdgpu_buffer_rsrc_t rsI = __builtin_amdgcn_make_buffer_rsrc((void*)(ws + WS_INBOX + (size_t)(b * NH + h) * 4096), 0, 4096, 0x00020000);
      const v4u inv = {0u, ~tag32, 0u, ~tag32};
#pragma unroll
      for (int i = 0; i < 4; ++i) __builtin_amdgcn_raw_buffer_store_b128(inv, rsI, lane * 16 + i * 1024, 0, 16);
      wait_vm<0>();
      if (lane == 0) __hip_atomic_store((GAS u64*)(ws + WS_READY) + b * NH + h, tag64, RLX_AGENT);
    }
    LAS float* xs = (LAS float*)(L + L_XS);
    { LAS f32x4* xs4 = (LAS f32x4*)xs + 2 * lane;
      xs4[0] = f32x4{bflo(xv.x), bfhi(xv.x), bflo(xv.y), bfhi(xv.y)}; xs4[1] = f32x4{bflo(xv.z), bfhi(xv.z), bflo(xv.w), bfhi(xv.w)}; }
    float sg = 0.f, sr = 0.f;
#pragma unroll
    for (int j = 0; j < 4; ++j) {
      sg += rbf(bflo(xv[j]) * bflo(gw[j])); sg += rbf(bfhi(xv[j]) * bfhi(gw[j]));
      sr = fmaf(bflo(xv[j]), bflo(wr[j]), sr); sr = fmaf(bfhi(xv[j]), bfhi(wr[j]), sr);
    }
    sg = rbf(wave_sum(sg)); sr = rbf(wave_sum(sr));
    const float g = rbf(sigmoidf_(sg)), rs = rbf(sigmoidf_(sr)), omg = rbf(1.0f - g);
    if (lane == 0) { LAS float* sca = (LAS float*)(L + L_SCAL); sca[0] = g; sca[1] = omg; }
    float y[8];
#pragma unroll
    for (int j = 0; j < 8; ++j) y[j] = 0.f;
    if (h < FF / FS) {
      wait_ge<8>(ctl + 1, (unsigned)NCONS);
      const int rr = lane >> 3, cc = lane & 7;
      const GAS v4u* gp = (const GAS v4u*)(w_gate + (size_t)(64 * rr) * FF + h * FS + 8 * cc);
      const GAS v4u* up = (const GAS v4u*)(w_up + (size_t)(64 * rr) * FF + h * FS + 8 * cc);
      float ag[8], au[8];
#pragma unroll
      for (int j = 0; j < 8; ++j) { ag[j] = 0.f; au[j] = 0.f; }
#pragma unroll 1
      for (int it = 0; it < 16; ++it) {
        v4u Gc[4], Uc[4];
#pragma unroll
        for (int t = 0; t < 4; ++t) { Gc[t] = gp[(it * 4 + t) * (FF / 8)]; Uc[t] = up[(it * 4 + t) * (FF / 8)]; }
        const f32x4 xq = *(const LAS f32x4*)(xs + 64 * rr + 4 * it);
#pragma unroll
        for (int t = 0; t < 4; ++t) {
          const float xt = xq[t];
#pragma unroll
          for (int j = 0; j < 4; ++j) {
            ag[2 * j] = fmaf(xt, bflo(Gc[t][j]), ag[2 * j]); ag[2 * j + 1] = fmaf(xt, bfhi(Gc[t][j]), ag[2 * j + 1]);
            au[2 * j] = fmaf(xt, bflo(Uc[t][j]), au[2 * j]); au[2 * j + 1] = fmaf(xt, bfhi(Uc[t][j]), au[2 * j + 1]);
          }
        }
        __builtin_amdgcn_s_sleep(MOE_SLEEP);
      }
      LAS float* act = (LAS float*)(L + L_ACT);
#pragma unroll
      for (int j = 0; j < 8; ++j) {
        float gg = ag[j], uu = au[j];
        gg += dpp<XOR8>(gg); uu += dpp<XOR8>(uu);
        gg = rbf(xrow_sum(gg)); uu = rbf(xrow_sum(uu));
        const float si = rbf(gg * sigmoidf_(gg));
        if (lane < 8) act[8 * lane + j] = rbf(si * uu);
      }
      const GAS v4u* dp = (const GAS v4u*)(w_down + (size_t)(h * FS) * DM + lane * 8);
#pragma unroll 1
      for (int it = 0; it < 8; ++it) {
        v4u Dc[8];
#pragma unroll
        for (int t = 0; t < 8; ++t) Dc[t] = dp[(it * 8 + t) * (DM / 8)];
#pragma unroll
        for (int t = 0; t < 8; ++t) {
          const float a = act[it * 8 + t];
#pragma unroll
          for (int j = 0; j < 4; ++j) { y[2 * j] = fmaf(a, bflo(Dc[t][j]), y[2 * j]); y[2 * j + 1] = fmaf(a, bfhi(Dc[t][j]), y[2 * j + 1]); }
        }
        __builtin_amdgcn_s_sleep(MOE_SLEEP);
      }
    }
    const float f = omg * rs;
    LAS f32x4* ym = (LAS f32x4*)(L + L_YMOE) + 2 * lane;
    ym[0] = f32x4{y[0] * f, y[1] * f, y[2] * f, y[3] * f}; ym[1] = f32x4{y[4] * f, y[5] * f, y[6] * f, y[7] * f};
    {
      GAS u64* rdy = (GAS u64*)(ws + WS_READY) + b * NH + (lane & 31);
      for (unsigned spins = 0; spins < SPIN_CAP; ++spins) {
        const u64 v = __hip_atomic_load(rdy, RLX_AGENT);
        if (__all(v == tag64)) break;
        __builtin_amdgcn_s_sleep(8);
      }
    }
    wait_lgkm();
    if (lane == 0) ctl[4] = 1u;
    raw_barrier();
  }
  {
    const u64 tag64 = ((const LAS u64*)(L + L_SCAL))[1];
    const unsigned tag32 = tag32_of(tag64);
    const float omg = ((const LAS float*)(L + L_SCAL))[1];
    GAS u64* src = (GAS u64*)(ws + WS_INBOX) + ((size_t)(b * NH + h) * NH + 8 * r) * 16 + c;
    const float xres = bflo((unsigned)xg[b * DM + 16 * h + c]);
    if (w == 0) __builtin_amdgcn_s_sleep(POLL_DELAY); else if (w == NCONS + 1) __builtin_amdgcn_s_sleep(POLL_DELAY + POLL_STAG); else __builtin_amdgcn_s_sleep(POLL_DELAY + 2 * POLL_STAG);
    for (unsigned spins = 0; spins < SPIN_CAP; ++spins) {
      bool ok = true;
      float s = 0.f;
#pragma unroll
      for (int i = 0; i < 8; ++i) { const u64 x = __hip_atomic_load(src + i * 16, RLX_AGENT); s += u2f((unsigned)x); ok &= (unsigned)(x >> 32) == tag32; }
      s = xrow_sum(s);
      s = fmaf(omg, xres, s);
      unsigned o2 = pack_bf16(s, dpp<XOR1>(s));
      asm volatile("" : "+v"(o2));
      if (__all(ok)) {
        if (lane < 16 && (lane & 1) == 0) __hip_atomic_store((GAS unsigned*)(out + b * DM + 16 * h + lane), o2, RLX_AGENT);
        break;
      }
    }
    if (w == 0 && bid == 0 && lane == 0) __hip_atomic_store((GAS u64*)(ws + WS_EPOCH), tag64, RLX_AGENT);
  }
}
}

extern "C" void kernel_launch(void* const* d_in, const int* in_sizes, int n_in, void* d_out, int out_size, void* d_ws, size_t ws_size, hipStream_t stream) {
  (void)in_sizes; (void)n_in; (void)out_size; (void)ws_size;
  (void)hipFuncSetAttribute((const void*)fused_kernel, hipFuncAttributeMaxDynamicSharedMemorySize, LDS_BYTES);
  fused_kernel<<<dim3(NB * NH), dim3(NTHR), LDS_BYTES, stream>>>(
      (const u16*)d_in[0], (const u16*)d_in[1], (const u16*)d_in[2], (const u16*)d_in[3], (const u16*)d_in[4],
      (const u16*)d_in[6], (const u16*)d_in[7], (const u16*)d_in[8], (const u16*)d_in[9], (const u16*)d_in[10], (u16*)d_out, (unsigned char*)d_ws);
}
```
